```python
import torch

def _softmax(x, axis=-1):
    return torch.softmax(x, dim=axis)
def _gelu(x):
    return torch.nn.functional.gelu(x, approximate="tanh")
def _silu(x):
    return torch.nn.functional.silu(x)
def _sigmoid(x):
    return torch.sigmoid(x)
def _layernorm(x, axis=-1, eps=1e-5):
    m = x.mean(dim=axis, keepdim=True)
    v = x.var(dim=axis, keepdim=True, unbiased=False)
    return (x - m) / torch.sqrt(v + eps)
def _scatter_add(tgt, idx, src, axis=0):
    return tgt.clone().index_add_(axis, idx, src)
def _segment_sum(src, seg, n):
    out = torch.zeros((n,) + src.shape[1:], dtype=src.dtype)
    return out.index_add_(0, seg, src)
def _pad(x, pw):
    flat = tuple(v for pair in reversed(pw) for v in pair)
    return torch.nn.functional.pad(x, flat)

kernel_name = 'sequential_preLN__sliding_local_dilated__glu_gated_glu__prompt'

def setup_inputs():

    _w = (lambda a: (lambda t: t.to(torch.bfloat16) if t.is_floating_point() else t)(torch.as_tensor(a)))
    def _A_setup():
        B, S, D = 1, 2048, 448
        return {
            "x": _w((lambda *s: torch.randn(*s, dtype=torch.bfloat16) * 0.05)(B, S, D)),
            "wqkv": _w((lambda *s: torch.randn(*s, dtype=torch.bfloat16) * 0.05)(D, 3 * D)),
            "wo": _w((lambda *s: torch.randn(*s, dtype=torch.bfloat16) * 0.05)(D, D)),
        }
    _A_in = _A_setup()

    def _B_setup():
        B, S, D, F = 1, 2048, 448, 1792
        return {
            "x": _w((lambda *s: torch.randn(*s, dtype=torch.bfloat16) * 0.05)(B, S, D)),
            "w_gate": _w((lambda *s: torch.randn(*s, dtype=torch.bfloat16) * 0.05)(D, F)),
            "w_up": _w((lambda *s: torch.randn(*s, dtype=torch.bfloat16) * 0.05)(D, F)),
            "w_down": _w((lambda *s: torch.randn(*s, dtype=torch.bfloat16) * 0.05)(F, D)),
        }
    _B_in = _B_setup()

    return {
        "A_x": _A_in["x"],
        "A_wqkv": _A_in["wqkv"],
        "A_wo": _A_in["wo"],
        "B_x": _B_in["x"],
        "B_w_gate": _B_in["w_gate"],
        "B_w_up": _B_in["w_up"],
        "B_w_down": _B_in["w_down"],
    }

def reference(A_x, A_wqkv, A_wo, B_x, B_w_gate, B_w_up, B_w_down):
    def _A_ref(x, wqkv, wo):
        B, S, D = x.shape
        H = 16; Dh = D // H
        W, dil = 8, 2
        qkv = (lambda x, *d: x.permute(*d))((x @ wqkv).reshape(B, S, 3, H, Dh), 2, 0, 3, 1, 4)
        q, k, v = qkv[0], qkv[1], qkv[2]
        scale = Dh ** -0.5
        pos = torch.arange(S)
        off = torch.arange(W) * dil
        idx = pos[:, None] - off[None, :]
        mask = idx >= 0
        idx_c = torch.where(mask, idx, 0).reshape(-1)
        kg = k[:, :, idx_c, :].reshape(B, H, S, W, Dh)
        vg = v[:, :, idx_c, :].reshape(B, H, S, W, Dh)
        logits = torch.einsum("bhsd,bhswd->bhsw", q, kg) * scale
        logits = torch.where(mask[None, None, :, :], logits, -1e9)
        attn = _softmax(logits, axis=-1)
        o = torch.einsum("bhsw,bhswd->bhsd", attn, vg)
        return (lambda x, *d: x.permute(*d))(o, 0, 2, 1, 3).reshape(B, S, D) @ wo

    def _B_ref(x, w_gate, w_up, w_down):
        g = torch.einsum("bsd,df->bsf", x, w_gate)
        u = torch.einsum("bsd,df->bsf", x, w_up)
        h = _sigmoid(g) * u
        return torch.einsum("bsf,fd->bsd", h, w_down)

    x = A_x
    h = x + _A_ref(_layernorm(x, axis=-1), A_wqkv, A_wo)
    h2 = h + _B_ref(_layernorm(h, axis=-1), B_w_gate, B_w_up, B_w_down)
    return h2
```

```cpp
#include <hip/hip_runtime.h>
#include <hip/hip_bf16.h>
typedef short bf16x8 __attribute__((ext_vector_type(8)));
typedef float f32x4 __attribute__((ext_vector_type(4)));
typedef unsigned short u16;
__device__ __forceinline__ float bf2f(u16 b){ return __uint_as_float(((unsigned)b)<<16); }
__device__ __forceinline__ u16 f2bf(float f){ unsigned u=__float_as_uint(f); u += 0x7fffu + ((u>>16)&1u); return (u16)(u>>16); }
constexpr int S=2048, D=448, F=1792, H=16, DH=28;

__global__ void ln_k(const u16* in, u16* out){
  int row=blockIdx.x; int t=threadIdx.x;
  float v[7]; float s=0;
  for(int i=0;i<7;i++){ v[i]=bf2f(in[row*D+t+64*i]); s+=v[i]; }
  for(int o=32;o;o>>=1) s+=__shfl_xor(s,o);
  float m=s/D; float q=0;
  for(int i=0;i<7;i++){ float d=v[i]-m; q+=d*d; }
  for(int o=32;o;o>>=1) q+=__shfl_xor(q,o);
  float r=rsqrtf(q/D+1e-5f);
  for(int i=0;i<7;i++) out[row*D+t+64*i]=f2bf((v[i]-m)*r);
}
template<int MODE>
__global__ void gemm_k(const u16* A, const u16* B, const u16* B2, const u16* res, u16* C, int N, int K){
  __shared__ u16 As[64][40]; __shared__ u16 Bs[64][40]; __shared__ u16 Bs2[64][40];
  int t=threadIdx.x, lane=t&63, w=t>>6;
  int m0=blockIdx.y*64, n0=blockIdx.x*64;
  f32x4 acc[4]={}, acc2[4]={};
  for(int k0=0;k0<K;k0+=32){
    { int r=t>>2, c=(t&3)*8; *(bf16x8*)&As[r][c] = *(const bf16x8*)&A[(m0+r)*K+k0+c]; }
    { int kk=t>>3, c=(t&7)*8; bf16x8 v=*(const bf16x8*)&B[(k0+kk)*N+n0+c]; for(int j=0;j<8;j++) Bs[c+j][kk]=v[j];
      if(MODE==2){ bf16x8 v2=*(const bf16x8*)&B2[(k0+kk)*N+n0+c]; for(int j=0;j<8;j++) Bs2[c+j][kk]=v2[j]; } }
    __syncthreads();
    bf16x8 a=*(bf16x8*)&As[w*16+(lane&15)][(lane>>4)*8];
    for(int n=0;n<4;n++){ bf16x8 b=*(bf16x8*)&Bs[n*16+(lane&15)][(lane>>4)*8];
      acc[n]=__builtin_amdgcn_mfma_f32_16x16x32_bf16(a,b,acc[n],0,0,0);
      if(MODE==2){ bf16x8 b2=*(bf16x8*)&Bs2[n*16+(lane&15)][(lane>>4)*8]; acc2[n]=__builtin_amdgcn_mfma_f32_16x16x32_bf16(a,b2,acc2[n],0,0,0);} }
    __syncthreads();
  }
  for(int n=0;n<4;n++) for(int j=0;j<4;j++){
    int row=m0+w*16+(lane>>4)*4+j, col=n0+n*16+(lane&15);
    float v=acc[n][j];
    if(MODE==1) v=bf2f(f2bf(v))+bf2f(res[row*N+col]);
    if(MODE==2){ v = acc2[n][j]/(1.f+__expf(-v)); }
    C[row*N+col]=f2bf(v);
  }
}
__global__ void attn_k(const u16* qkv, u16* o){
  int idx=blockIdx.x*blockDim.x+threadIdx.x; int s=idx>>4, h=idx&15;
  float q[DH]; for(int d=0;d<DH;d++) q[d]=bf2f(qkv[s*3*D+h*DH+d]);
  float l[8]; float mx=-1e30f;
  for(int w=0;w<8;w++){ int j=s-2*w; float a=0; if(j>=0){ for(int d=0;d<DH;d++) a+=q[d]*bf2f(qkv[j*3*D+D+h*DH+d]); a*=0.18898223650461362f;} else a=-1e9f; l[w]=a; mx=fmaxf(mx,a);}
  float sum=0; for(int w=0;w<8;w++){ l[w]=__expf(l[w]-mx); sum+=l[w]; }
  float inv=1.f/sum;
  for(int d=0;d<DH;d++){ float a=0; for(int w=0;w<8;w++){ int j=s-2*w; if(j<0) j=0; a+=l[w]*bf2f(qkv[j*3*D+2*D+h*DH+d]); } o[s*D+h*DH+d]=f2bf(a*inv);}
}
extern "C" void kernel_launch(void* const* d_in, const int* in_sizes, int n_in, void* d_out, int out_size, void* d_ws, size_t ws_size, hipStream_t stream){
  const u16* x=(const u16*)d_in[0]; const u16* wqkv=(const u16*)d_in[1]; const u16* wo=(const u16*)d_in[2];
  const u16* wg=(const u16*)d_in[4]; const u16* wu=(const u16*)d_in[5]; const u16* wd=(const u16*)d_in[6];
  u16* ws=(u16*)d_ws; u16* xn=ws; u16* qkv=xn+S*D; u16* o=qkv+S*3*D; u16* h=o+S*D; u16* hn=h+S*D; u16* hh=hn+S*D;
  ln_k<<<S,64,0,stream>>>(x,xn);
  gemm_k<0><<<dim3(3*D/64,S/64),256,0,stream>>>(xn,wqkv,nullptr,nullptr,qkv,3*D,D);
  attn_k<<<S*H/256,256,0,stream>>>(qkv,o);
  gemm_k<1><<<dim3(D/64,S/64),256,0,stream>>>(o,wo,nullptr,x,h,D,D);
  ln_k<<<S,64,0,stream>>>(h,hn);
  gemm_k<2><<<dim3(F/64,S/64),256,0,stream>>>(hn,wg,wu,nullptr,hh,F,D);
  gemm_k<1><<<dim3(D/64,S/64),256,0,stream>>>(hh,wd,nullptr,h,(u16*)d_out,D,F);
}
```

```cpp
#include <hip/hip_runtime.h>
#include <hip/hip_bf16.h>

typedef unsigned u32;
typedef unsigned long long u64;
typedef u32 v4u __attribute__((ext_vector_type(4)));
typedef u32 v2u __attribute__((ext_vector_type(2)));
typedef short s16x4 __attribute__((ext_vector_type(4)));
typedef short s16x8 __attribute__((ext_vector_type(8)));
typedef __bf16 bf16x2 __attribute__((ext_vector_type(2)));
typedef __bf16 bf16x8 __attribute__((ext_vector_type(8)));
typedef float f32x2 __attribute__((ext_vector_type(2)));
typedef float f32x4 __attribute__((ext_vector_type(4)));
typedef float f32x16 __attribute__((ext_vector_type(16)));
#define GAS __attribute__((address_space(1)))
#define LAS __attribute__((address_space(3)))
#define DI __device__ __forceinline__
typedef __amdgpu_buffer_rsrc_t rsrc_t;

#define STAMP 0

constexpr u32 WS_EPOCH = 0;
constexpr u32 WS_FLAG = 4096;
constexpr u32 WS_O = 512 * 1024;
constexpr u32 WS_H = WS_O + 2 * 1024 * 1024;
constexpr u32 WS_ST = WS_H + 64 * 28 * 1024;
constexpr u32 WS_HH = WS_ST + 128 * 1024;
constexpr u32 WS_END = WS_HH + 64 * 112 * 1024;
constexpr u32 WS_DBG = 16 * 1024 * 1024;
constexpr int FL1 = 0, FL2 = 512, FL3 = 1024;

constexpr u32 PL = 14368;
constexpr u32 L_XB = 6 * PL;
constexpr u32 XROW = 928, XBLK = 16 * XROW;
constexpr u32 L_XH = L_XB + 4 * XBLK;
constexpr u32 L_EX = L_XH + 8 * XROW;
constexpr u32 L_CS1 = L_EX + 5 * 2048;
constexpr u32 L_CNT = L_CS1 + 384;
constexpr u32 L_P2 = 8 * PL;
constexpr u32 L_CS3 = L_EX;
constexpr u32 PROW = 272;
constexpr u32 W2PL = 3616, W4PL = 3648;
constexpr u32 LDS_BYTES = 163840;

#define WAITV(n) asm volatile("s_waitcnt vmcnt(" #n ")" ::: "memory")
#define BAR() asm volatile("s_waitcnt lgkmcnt(0)\n\ts_barrier" ::: "memory")
#define SB() __builtin_amdgcn_sched_barrier(0)

DI void dma16(u32 lds, u32 voff, rsrc_t rs, u32 soff) {
  asm volatile("s_mov_b32 m0, %0\n\ts_nop 0\n\tbuffer_load_dwordx4 %1, %2, %3 offen lds" ::"s"(lds), "v"(voff), "s"(rs), "s"(soff)
               : "memory");
}
DI rsrc_t mkrs(const void* p, u32 bytes) { return __builtin_amdgcn_make_buffer_rsrc((void*)p, (short)0, (int)bytes, 0x00020000); }
DI v4u trfrag(LAS char* a, LAS char* b) {
  s16x4 lo = __builtin_amdgcn_ds_read_tr16_b64_v4i16((LAS s16x4*)a);
  s16x4 hi = __builtin_amdgcn_ds_read_tr16_b64_v4i16((LAS s16x4*)b);
  return __builtin_bit_cast(v4u, __builtin_shufflevector(lo, hi, 0, 1, 2, 3, 4, 5, 6, 7));
}
DI f32x4 mfma16(v4u a, v4u b, f32x4 c) {
  return __builtin_amdgcn_mfma_f32_16x16x32_bf16(__builtin_bit_cast(bf16x8, a), __builtin_bit_cast(bf16x8, b), c, 0, 0, 0);
}
DI f32x16 mfma32(v4u a, v4u b, f32x16 c) {
  return __builtin_amdgcn_mfma_f32_32x32x16_bf16(__builtin_bit_cast(bf16x8, a), __builtin_bit_cast(bf16x8, b), c, 0, 0, 0);
}
DI u32 pk(float a, float b) {
  f32x2 f = {a, b};
  return __builtin_bit_cast(u32, __builtin_convertvector(f, bf16x2));
}
DI float bflo(u32 u) { return __builtin_bit_cast(float, u << 16); }
DI float bfhi(u32 u) { return __builtin_bit_cast(float, u & 0xffff0000u); }
DI float dot2(u32 a, u32 b, float c) {
  return __builtin_amdgcn_fdot2_f32_bf16(__builtin_bit_cast(bf16x2, a), __builtin_bit_cast(bf16x2, b), c, false);
}
DI float sum16(float x) {
  auto r = __builtin_amdgcn_permlane16_swap(__builtin_bit_cast(u32, x), __builtin_bit_cast(u32, x), false, false);
  const u32 a = r[0], c = r[1];
  return __uint_as_float(a) + __uint_as_float(c);
}
DI float sum32(float x) {
  auto r = __builtin_amdgcn_permlane32_swap(__builtin_bit_cast(u32, x), __builtin_bit_cast(u32, x), false, false);
  const u32 a = r[0], c = r[1];
  return __uint_as_float(a) + __uint_as_float(c);
}
DI float max16(float x) {
  auto r = __builtin_amdgcn_permlane16_swap(__builtin_bit_cast(u32, x), __builtin_bit_cast(u32, x), false, false);
  const u32 a = r[0], c = r[1];
  return fmaxf(__uint_as_float(a), __uint_as_float(c));
}
DI float max32(float x) {
  auto r = __builtin_amdgcn_permlane32_swap(__builtin_bit_cast(u32, x), __builtin_bit_cast(u32, x), false, false);
  const u32 a = r[0], c = r[1];
  return fmaxf(__uint_as_float(a), __uint_as_float(c));
}
DI float bperm(int srclane, float x) {
  return __builtin_bit_cast(float, __builtin_amdgcn_ds_bpermute(srclane * 4, __builtin_bit_cast(int, x)));
}
#define RLX_AGENT __ATOMIC_RELAXED, __HIP_MEMORY_SCOPE_AGENT
DI void poll(char* ws, int idx, bool act, u64 tag) {
  GAS u64* p = (GAS u64*)(ws + WS_FLAG + (u32)idx * 128u);
  for (int spins = 0; spins < (1 << 16); ++spins) {
    u64 v = tag;
    if (act) v = __hip_atomic_load(p, RLX_AGENT);
    if (__all(v == tag)) break;
    __builtin_amdgcn_s_sleep(1);
  }
  asm volatile("" ::: "memory");
}
DI void setflag(char* ws, int idx, u64 tag) { __hip_atomic_store((GAS u64*)(ws + WS_FLAG + (u32)idx * 128u), tag, RLX_AGENT); }
DI void publish4(LAS char* L, int cnt, char* ws, int idx, u64 tag, int lane) {
  WAITV(0);
  if (lane == 0) {
    u32 old = __hip_atomic_fetch_add((LAS u32*)(L + L_CNT + cnt * 4), 1u, __ATOMIC_RELAXED, __HIP_MEMORY_SCOPE_WORKGROUP);
    if (old == 3) setflag(ws, idx, tag);
  }
}
#define TS(i)
#define TSOUT()

__global__ __launch_bounds__(512, 2) void fused_block(const void* __restrict__ xg, const void* __restrict__ wqkv,
                                                      const void* __restrict__ wo, const void* __restrict__ wg,
                                                      const void* __restrict__ wu, const void* __restrict__ wd,
                                                      void* __restrict__ outg, char* ws) {
  __shared__ __attribute__((aligned(16))) char smem[LDS_BYTES];
  LAS char* L = (LAS char*)smem;
  const u32 L0 = (u32)(size_t)L;
  const int b = blockIdx.x, xcd = b & 7, jj = b >> 3;
  const int lane = threadIdx.x & 63, w = __builtin_amdgcn_readfirstlane(threadIdx.x >> 6);
  const int r16 = lane & 15, g = lane >> 4, tq = (lane >> 2) & 3, tp = lane & 3, hh = lane >> 5;
  TS(0);
  const rsrc_t rsX = mkrs(xg, 2048 * 896), rsW1 = mkrs(wqkv, 448 * 2688), rsWS = mkrs(ws, WS_END);

  const int tile1 = (xcd >> 1) * 4 + (jj & 3), head = (xcd & 1) * 8 + (jj >> 2);
  const int srow0 = 256 * (tile1 >> 1) + (tile1 & 1);
  v2u epoch;
  asm volatile("buffer_load_dwordx2 %0, off, %1, 0 sc1" : "=v"(epoch) : "s"(rsWS) : "memory");
  if (lane < 56) {
    _Pragma("unroll 1") for (int k = 0; k < 9; ++k) {
      const int n = 9 * w + k;
      int s = srow0 + 2 * (n < 64 ? n : n - 72);
      s = s < 0 ? 0 : s;
      dma16(L0 + L_XB + n * XROW, lane * 16, rsX, s * 896);
    }
  }
  {
    const u32 vo = (16 * (lane >> 5) + ((lane >> 2) & 7)) * 2688 + (lane & 3) * 16;
    const u32 w0 = (56 * head) & ~15;
    _Pragma("unroll 1") for (int k = 0; k < 11; ++k) {
      int n = k < 9 ? 24 * (k / 3) + 3 * w + (k % 3) : 72 + (2 * w + (k - 9)) % 12;
      const int ks = n / 6, e = n % 6;
      dma16(L0 + e * PL + ks * 1024, vo, rsW1, (32 * ks + 8 * (e & 1)) * 2688 + (e >> 1) * 896 + w0);
    }
  }
  if (threadIdx.x < 8) *(LAS u32*)(L + L_CNT + threadIdx.x * 4) = 0;
  const bool tail = jj < 28;
  const int tt2 = jj & 3, c2 = jj >> 2;
  const int T3 = (xcd >> 2) * 4 + (jj & 3), fs3 = (xcd & 3) * 7 + (jj >> 2);
  const rsrc_t rsWo = mkrs(wo, 448 * 896);
  TS(1);
  asm volatile("s_waitcnt vmcnt(8)" : "+v"(epoch)::"memory");
  BAR();
  u64 tag;
  {
    const u32 lo = __builtin_amdgcn_readfirstlane(epoch[0]), hi = __builtin_amdgcn_readfirstlane(epoch[1]);
    tag = (((u64)hi << 32) | lo) * 6364136223846793005ull + 1442695040888963407ull;
  }

#pragma unroll 1
  for (int round = 0; round < 2; ++round) {
    const bool active = round == 0 ? (w <= 4) : (w >= 4);
    if (active) {
      const bool halo = (round == 0) && (w == 4);
      if (round == 1) WAITV(0);
      TS(2 + 8 * round);
      v4u xf[14];
      {
        LAS char* xa = halo ? L + L_XH + (r16 & 7) * XROW + g * 16 : L + L_XB + (w & 3) * XBLK + r16 * XROW + g * 16;
#pragma unroll
        for (int ks = 0; ks < 14; ++ks) xf[ks] = *(LAS v4u*)(xa + ks * 64);
        if (halo && r16 == 0) {
#pragma unroll
          for (int ks = 0; ks < 14; ++ks) xf[ks] = v4u{0x3f803f80u, 0x3f803f80u, 0x3f803f80u, 0x3f803f80u};
        }
      }
      if (round == 1 && tail) {
        asm volatile("s_waitcnt lgkmcnt(0)" ::: "memory");
        const u32 vo = (lane >> 3) * 4 * 896 + (lane & 7) * 16;
        _Pragma("unroll 1") for (int k = 0; k < 16; ++k) {
          const int q = k & 3, part = k >> 2;
          if (part < 3 || lane < 32)
            dma16(L0 + L_XB + (w - 4) * XBLK + q * W2PL + part * 1024, vo, rsWo, (112 * (w - 4) + 32 * part + q) * 896 + c2 * 128);
        }
      }
      f32x4 acc[6];
#pragma unroll
      for (int i = 0; i < 6; ++i) acc[i] = f32x4{0.f, 0.f, 0.f, 0.f};
      float s1 = 0.f, s2 = 0.f;
      LAS char* wa = L + (g & 1) * PL + (g >> 1) * 512 + tq * 64 + tp * 8 + (head & 1) * 8;
#pragma unroll
      for (int ks = 0; ks < 14; ++ks) {
        if (ks == 4 && round == 0) {
          WAITV(5);
          BAR();
          if (w == 4 && lane < 56)
            _Pragma("unroll 1") for (int k = 0; k < 16; ++k) dma16(L0 + L_XB + k * XROW, lane * 16, rsX, (srow0 + 2 * (64 + k)) * 896);
        }
        if (ks == 8 && round == 0) {
          if (w == 4) WAITV(18); else WAITV(2);
          BAR();
        }
        if (ks == 12 && round == 0) {
          if (w == 4) WAITV(16); else WAITV(0);
          BAR();
        }
#pragma unroll
        for (int i = 0; i < 6; ++i) {
          LAS char* a = wa + (i >> 1) * 2 * PL + ks * 1024 + (i & 1) * 32;
          const v4u wf = trfrag(a, a + 256);
          acc[i] = i < 4 ? mfma16(wf, xf[ks], acc[i]) : mfma16(xf[ks], wf, acc[i]);
        }
#pragma unroll
        for (int e = 0; e < 4; ++e) {
          s1 = dot2(xf[ks][e], 0x3f803f80u, s1);
          s2 = dot2(xf[ks][e], xf[ks][e], s2);
        }
      }
      TS(3 + 8 * round);
      if (round == 0) {
        if (halo) {
          if (r16 == 0) {
#pragma unroll
            for (int i = 0; i < 4; ++i) *(LAS f32x4*)(L + L_CS1 + (16 * i + 4 * g) * 4) = acc[i];
          }
          if (g == 0) {
            *(LAS float*)(L + L_CS1 + (64 + r16) * 4) = acc[4][0];
            *(LAS float*)(L + L_CS1 + (80 + r16) * 4) = acc[5][0];
          }
        }
        BAR();
      }
      s1 = sum32(sum16(s1));
      s2 = sum32(sum16(s2));
      const float mean = s1 * (1.f / 448.f);
      const float rstd = __builtin_amdgcn_rsqf(fmaxf(s2 * (1.f / 448.f) - mean * mean, 0.f) + 1e-5f);
      v4u qf, kf, vf;
      {
        const float rq = rstd * (0.18898223650461362f * 1.4426950408889634f);
        float t[4][4];
#pragma unroll
        for (int i = 0; i < 4; ++i) {
          const f32x4 cs = *(LAS f32x4*)(L + L_CS1 + (16 * i + 4 * g) * 4);
#pragma unroll
          for (int j = 0; j < 4; ++j) t[i][j] = (acc[i][j] - mean * cs[j]) * (i < 2 ? rq : rstd);
        }
        qf = v4u{pk(t[0][0], t[0][1]), pk(t[0][2], t[0][3]), g == 3 ? 0u : pk(t[1][0], t[1][1]), g == 3 ? 0u : pk(t[1][2], t[1][3])};
        kf = v4u{pk(t[2][0], t[2][1]), pk(t[2][2], t[2][3]), g == 3 ? 0u : pk(t[3][0], t[3][1]), g == 3 ? 0u : pk(t[3][2], t[3][3])};
        const float c0 = *(LAS float*)(L + L_CS1 + (64 + r16) * 4), c1 = *(LAS float*)(L + L_CS1 + (80 + r16) * 4);
        float v0[4], v1[4];
#pragma unroll
        for (int j = 0; j < 4; ++j) {
          const float mj = bperm(4 * g + j, mean), rj = bperm(4 * g + j, rstd);
          v0[j] = (acc[4][j] - mj * c0) * rj;
          v1[j] = (acc[5][j] - mj * c1) * rj;
        }
        vf = v4u{pk(v0[0], v0[1]), pk(v0[2], v0[3]), pk(v1[0], v1[1]), pk(v1[2], v1[3])};
      }
      if (w != 7) {
        const u32 wr = round == 0 ? L_EX + (halo ? 0 : w + 1) * 2048 : L_XH + (w - 4) * 2048;
        *(LAS v4u*)(L + wr + lane * 16) = kf;
        *(LAS v4u*)(L + wr + 1024 + lane * 16) = vf;
      }
      BAR();
      TS(4 + 8 * round);
      if (!halo) {
        const u32 rd = w <= 4 ? L_EX + w * 2048 : L_XH + (w - 5) * 2048;
        const v4u kp = *(LAS v4u*)(L + rd + lane * 16), vp = *(LAS v4u*)(L + rd + 1024 + lane * 16);
        const f32x4 z = {0.f, 0.f, 0.f, 0.f};
        const f32x4 sp = mfma16(kp, qf, z), so = mfma16(kf, qf, z);
        const bool noprev = (tile1 < 2) && (w == 0);
        float p[8];
        float mx = -3.0e38f;
#pragma unroll
        for (int j = 0; j < 4; ++j) {
          const int d = r16 - (4 * g + j);
          p[j] = (d <= -9 && !noprev) ? sp[j] : -3.0e38f;
          p[4 + j] = (d >= 0 && d <= 7) ? so[j] : -3.0e38f;
          mx = fmaxf(mx, fmaxf(p[j], p[4 + j]));
        }
        mx = max32(max16(mx));
        float sum = 0.f;
#pragma unroll
        for (int j = 0; j < 8; ++j) {
          p[j] = __builtin_amdgcn_exp2f(p[j] - mx);
          sum += p[j];
        }
        sum = sum32(sum16(sum));
        const v4u pf = {pk(p[0], p[1]), pk(p[2], p[3]), pk(p[4], p[5]), pk(p[6], p[7])};
        const f32x4 o0 = mfma16(v4u{vp[0], vp[1], vf[0], vf[1]}, pf, z), o1 = mfma16(v4u{vp[2], vp[3], vf[2], vf[3]}, pf, z);
        const float inv = __builtin_amdgcn_rcpf(sum);
        const v4u of = {pk(o0[0] * inv, o0[1] * inv), pk(o0[2] * inv, o0[3] * inv), g == 3 ? 0u : pk(o1[0] * inv, o1[1] * inv),
                        g == 3 ? 0u : pk(o1[2] * inv, o1[3] * inv)};
        __builtin_amdgcn_raw_buffer_store_b128(of, rsWS, lane * 16, WS_O + ((tile1 * 8 + w) * 16 + head) * 1024, 16);
        TS(5 + 8 * round);
        publish4(L, round, ws, FL1 + (tile1 * 16 + head) * 2 + round, tag, lane);
        TS(6 + 8 * round);
      }
    } else if (round == 0) {
      WAITV(5);
      BAR();
      if (lane < 56)
        _Pragma("unroll 1") for (int k = 0; k < 16; ++k) dma16(L0 + L_XB + ((w - 4) * 16 + k) * XROW, lane * 16, rsX, (srow0 + 2 * (16 * w + k)) * 896);
      WAITV(18);
      BAR();
      WAITV(16);
      BAR();
      BAR();
      BAR();
    } else {
      BAR();
    }
  }
  if (!tail) {
    if (b == 255 && w < 4) {
      poll(ws, FL1 + (w * 64 + lane) * 2 + 1, true, tag);
      if (lane == 0) {
        u32 old = __hip_atomic_fetch_add((LAS u32*)(L + L_CNT + 16), 1u, __ATOMIC_RELAXED, __HIP_MEMORY_SCOPE_WORKGROUP);
        if (old == 3) __hip_atomic_store((GAS u64*)(ws + WS_EPOCH), tag, RLX_AGENT);
      }
    }
    TSOUT();
    return;
  }

  const rsrc_t rsWg = mkrs(wg, 448 * 3584), rsWu = mkrs(wu, 448 * 3584);
  const u32 vo3 = (lane >> 3) * 4 * 3584 + (lane & 7) * 16;
  if (w < 4) {
    _Pragma("unroll 1") for (int k = 0; k < 21; ++k) {
      const int n = 21 * w + k, pl = n / 14, part = n % 14;
      dma16(L0 + pl * PL + part * 1024, vo3, pl < 4 ? rsWg : rsWu, (32 * part + (pl & 3)) * 3584 + fs3 * 128);
    }
  }
  const int p2 = tt2 >> 1, q2 = tt2 & 1, stw = w >> 2;
  const int ftok = 8 * (w & 3) + (lane & 7), fgr = lane >> 3;
  const u32 frow = (256 * xcd + 2 * (64 * stw + 32 * q2 + ftok) + p2) * 896 + c2 * 128 + fgr * 16;
  const v4u xres = __builtin_amdgcn_raw_buffer_load_b128(rsX, frow, 0, 0);
  v4u hres;
  const int rb = w & 1, kq = w >> 1;
  if (w == 0) poll(ws, FL1 + ((xcd * 2 + p2) * 16 + lane) * 2, lane < 16, tag);
  TS(16);
  BAR();
  TS(17);
  v4u w2f[4][4];
#pragma unroll 1
  for (int st = 0; st < 2; ++st) {
    v4u of[4];
#pragma unroll
    for (int hd = 0; hd < 4; ++hd)
      of[hd] = __builtin_amdgcn_raw_buffer_load_b128(
          rsWS, lane * 16, WS_O + (((xcd * 2 + p2) * 8 + 4 * st + 2 * q2 + rb) * 16 + 4 * kq + hd) * 1024, 16);
    SB();
    if (st == 0) {
      LAS char* a = L + L_XB + kq * XBLK + tq * W2PL + g * 128 + tp * 8;
      LAS char* a2 = a + (g < 3 ? 512 : 0);
#pragma unroll
      for (int hd = 0; hd < 4; ++hd)
#pragma unroll
        for (int cb = 0; cb < 4; ++cb) w2f[hd][cb] = trfrag(a + hd * 896 + cb * 32, a2 + hd * 896 + cb * 32);
      BAR();
    } else if (w < 4) {
      LAS char* a = L + (4 * (g & 1) + tq) * PL + hh * 256 + tp * 8 + w * 32;
      f32x16 cs;
#pragma unroll
      for (int i = 0; i < 16; ++i) cs[i] = 0.f;
      const v4u ones = {0x3f803f80u, 0x3f803f80u, 0x3f803f80u, 0x3f803f80u};
#pragma unroll 4
      for (int ks = 0; ks < 28; ++ks) cs = mfma32(trfrag(a + ks * 512, a + ks * 512 + 128), ones, cs);
      if ((lane & 31) == 0) {
#pragma unroll
        for (int i = 0; i < 4; ++i)
          *(LAS f32x4*)(L + L_CS3 + (w * 2 + hh) * 64 + i * 16) = f32x4{cs[4 * i], cs[4 * i + 1], cs[4 * i + 2], cs[4 * i + 3]};
      }
    }
    f32x4 acc[4];
#pragma unroll
    for (int cb = 0; cb < 4; ++cb) {
      acc[cb] = f32x4{0.f, 0.f, 0.f, 0.f};
#pragma unroll
      for (int hd = 0; hd < 4; ++hd) acc[cb] = mfma16(w2f[hd][cb], of[hd], acc[cb]);
      *(LAS f32x4*)(L + L_P2 + (kq * 32 + 16 * rb + r16) * PROW + (16 * cb + 4 * g) * 4) = acc[cb];
    }
    BAR();
    TS(18 + 3 * st);
    if (stw == st) {
      float v[8];
      LAS char* a = L + L_P2 + ftok * PROW + fgr * 32;
#pragma unroll
      for (int i = 0; i < 4; ++i) {
        v[2 * i] = bflo(xres[i]);
        v[2 * i + 1] = bfhi(xres[i]);
      }
#pragma unroll
      for (int k = 0; k < 4; ++k) {
        const f32x4 lo = *(LAS f32x4*)(a + k * 32 * PROW), hi = *(LAS f32x4*)(a + k * 32 * PROW + 16);
#pragma unroll
        for (int i = 0; i < 4; ++i) {
          v[i] += lo[i];
          v[4 + i] += hi[i];
        }
      }
      hres = v4u{pk(v[0], v[1]), pk(v[2], v[3]), pk(v[4], v[5]), pk(v[6], v[7])};
      const int t32 = (xcd * 2 + st) * 4 + tt2;
      __builtin_amdgcn_raw_buffer_store_b128(hres, rsWS, (fgr >> 1) * 1024 + ((fgr & 1) * 32 + ftok) * 16,
                                             WS_H + (t32 * 28 + 4 * c2) * 1024, 16);
      float s1 = 0.f, s2 = 0.f;
#pragma unroll
      for (int i = 0; i < 4; ++i) {
        s1 = dot2(hres[i], 0x3f803f80u, s1);
        s2 = dot2(hres[i], hres[i], s2);
      }
      s1 += __shfl_xor(s1, 8);
      s2 += __shfl_xor(s2, 8);
      s1 = sum32(sum16(s1));
      s2 = sum32(sum16(s2));
      if (lane < 8)
        __builtin_amdgcn_raw_buffer_store_b64(v2u{__builtin_bit_cast(u32, s1), __builtin_bit_cast(u32, s2)}, rsWS, ftok * 8,
                                              WS_ST + (t32 * 7 + c2) * 256, 16);
      TS(19 + 3 * st);
      publish4(L, 2 + st, ws, FL2 + t32 * 7 + c2, tag, lane);
      TS(20 + 3 * st);
    } else if (st == 0) {
      _Pragma("unroll 1") for (int k = 0; k < 7; ++k) {
        const int n = 7 * (w - 4) + k, pl = 6 + n / 14, part = n % 14;
        dma16(L0 + pl * PL + part * 1024, vo3, rsWu, (32 * part + (pl & 3)) * 3584 + fs3 * 128);
      }
      if (w == 7) poll(ws, FL1 + ((xcd * 2 + p2) * 16 + lane) * 2 + 1, lane < 16, tag);
      WAITV(0);
    }
    if (st == 0) BAR();
  }

  {
    const int t32 = (T3 * 2 + stw) * 4 + (w & 3);
    poll(ws, FL2 + t32 * 7 + lane, lane < 7, tag);
    TS(24);
    v4u hb[28];
#pragma unroll
    for (int ks = 0; ks < 28; ++ks) hb[ks] = __builtin_amdgcn_raw_buffer_load_b128(rsWS, lane * 16, WS_H + (t32 * 28 + ks) * 1024, 16);
    v2u sv[4];
#pragma unroll
    for (int i = 0; i < 4; ++i)
      sv[i] = __builtin_amdgcn_raw_buffer_load_b64(rsWS, (i < 3 ? hh : 0) * 256 + (lane & 31) * 8, WS_ST + (t32 * 7 + 2 * i) * 256, 16);
    SB();
    f32x16 acc[4];
#pragma unroll
    for (int c = 0; c < 4; ++c)
#pragma unroll
      for (int i = 0; i < 16; ++i) acc[c][i] = 0.f;
    LAS char* a = L + (4 * (g & 1) + tq) * PL + hh * 256 + tp * 8;
#pragma unroll
    for (int ks = 0; ks < 28; ++ks) {
#pragma unroll
      for (int c = 0; c < 4; ++c) acc[c] = mfma32(trfrag(a + ks * 512 + c * 32, a + ks * 512 + c * 32 + 128), hb[ks], acc[c]);
    }
    TS(25);
    float s1 = 0.f, s2 = 0.f;
#pragma unroll
    for (int i = 0; i < 3; ++i) {
      s1 += __uint_as_float(sv[i][0]);
      s2 += __uint_as_float(sv[i][1]);
    }
    s1 = sum32(s1) + __uint_as_float(sv[3][0]);
    s2 = sum32(s2) + __uint_as_float(sv[3][1]);
    const float mean = s1 * (1.f / 448.f);
    const float rstd = __builtin_amdgcn_rsqf(fmaxf(s2 * (1.f / 448.f) - mean * mean, 0.f) + 1e-5f);
    const float ng = -rstd * 1.4426950408889634f;
#pragma unroll
    for (int c = 0; c < 4; ++c) {
      float o[8];
#pragma unroll
      for (int i = 0; i < 2; ++i) {
        const f32x4 cg = *(LAS f32x4*)(L + L_CS3 + (c * 2 + hh) * 64 + i * 16), cu = *(LAS f32x4*)(L + L_CS3 + (c * 2 + hh) * 64 + 32 + i * 16);
#pragma unroll
        for (int j = 0; j < 4; ++j) {
          const float e = __builtin_amdgcn_exp2f((acc[c][4 * i + j] - mean * cg[j]) * ng);
          o[4 * i + j] = (acc[c][8 + 4 * i + j] - mean * cu[j]) * rstd * __builtin_amdgcn_rcpf(1.f + e);
        }
      }
      __builtin_amdgcn_raw_buffer_store_b128(v4u{pk(o[0], o[1]), pk(o[2], o[3]), pk(o[4], o[5]), pk(o[6], o[7])}, rsWS, lane * 16,
                                             WS_HH + (t32 * 112 + fs3 * 4 + c) * 1024, 16);
    }
    TS(26);
    WAITV(0);
    if (lane == 0) setflag(ws, FL3 + t32 * 28 + fs3, tag);
    TS(27);
  }
  BAR();

  {
    const rsrc_t rsWd = mkrs(wd, 1792 * 896);
    v4u w4f[14][2];
    const u32 vo = (lane >> 3) * 4 * 896 + (lane & 7) * 16;
    LAS char* a = L + w * (4 * W4PL) + tq * W4PL + hh * 128 + (g & 1) * 32 + tp * 8;
#pragma unroll
    for (int rr = 0; rr < 2; ++rr) {
      _Pragma("unroll 1") for (int k = 0; k < 16; ++k) {
        const int q = k & 3, part = k >> 2;
        if (part < 3 || lane < 32)
          dma16(L0 + w * (4 * W4PL) + q * W4PL + part * 1024, vo, rsWd, (224 * w + 112 * rr + 32 * part + q) * 896 + c2 * 128);
      }
      WAITV(0);
#pragma unroll
      for (int kk = 0; kk < 7; ++kk)
#pragma unroll
        for (int ct = 0; ct < 2; ++ct) w4f[7 * rr + kk][ct] = trfrag(a + kk * 512 + ct * 64, a + kk * 512 + ct * 64 + 256);
      asm volatile("s_waitcnt lgkmcnt(0)" ::: "memory");
    }
    TS(28);
    f32x16 acc[2][2];
#pragma unroll
    for (int st = 0; st < 2; ++st) {
      const int t32 = (xcd * 2 + st) * 4 + tt2;
      const int f0 = (14 * w) >> 2, f1 = (14 * w + 13) >> 2;
      poll(ws, FL3 + t32 * 28 + f0 + lane, lane <= f1 - f0, tag);
      TS(29 + 2 * st);
      v4u hf[14];
#pragma unroll
      for (int kk = 0; kk < 14; ++kk)
        hf[kk] = __builtin_amdgcn_raw_buffer_load_b128(rsWS, lane * 16, WS_HH + (t32 * 112 + 14 * w + kk) * 1024, 16);
      SB();
#pragma unroll
      for (int ct = 0; ct < 2; ++ct)
#pragma unroll
        for (int i = 0; i < 16; ++i) acc[st][ct][i] = 0.f;
#pragma unroll
      for (int kk = 0; kk < 14; ++kk)
#pragma unroll
        for (int ct = 0; ct < 2; ++ct) acc[st][ct] = mfma32(w4f[kk][ct], hf[kk], acc[st][ct]);
      TS(30 + 2 * st);
    }
    BAR();
#pragma unroll
    for (int st = 0; st < 2; ++st)
#pragma unroll
      for (int ct = 0; ct < 2; ++ct)
#pragma unroll
        for (int i = 0; i < 4; ++i)
          *(LAS f32x4*)(L + ((st * 8 + w) * 32 + (lane & 31)) * PROW + (32 * ct + 8 * i + 4 * hh) * 4) =
              f32x4{acc[st][ct][4 * i], acc[st][ct][4 * i + 1], acc[st][ct][4 * i + 2], acc[st][ct][4 * i + 3]};
    BAR();
    TS(33);
    float v[8];
#pragma unroll
    for (int i = 0; i < 4; ++i) {
      v[2 * i] = bflo(hres[i]);
      v[2 * i + 1] = bfhi(hres[i]);
    }
    LAS char* pa = L + (stw * 8 * 32 + ftok) * PROW + fgr * 32;
#pragma unroll
    for (int k = 0; k < 8; ++k) {
      const f32x4 lo = *(LAS f32x4*)(pa + k * 32 * PROW), hi = *(LAS f32x4*)(pa + k * 32 * PROW + 16);
#pragma unroll
      for (int i = 0; i < 4; ++i) {
        v[i] += lo[i];
        v[4 + i] += hi[i];
      }
    }
    *(GAS v4u*)((GAS char*)outg + frow) = v4u{pk(v[0], v[1]), pk(v[2], v[3]), pk(v[4], v[5]), pk(v[6], v[7])};
    TS(34);
  }
  TSOUT();
}

extern "C" void kernel_launch(void* const* d_in, const int* in_sizes, int n_in, void* d_out, int out_size, void* d_ws,
                              size_t ws_size, hipStream_t stream) {
  fused_block<<<256, 512, 0, stream>>>(d_in[0], d_in[1], d_in[2], d_in[4], d_in[5], d_in[6], d_out, (char*)d_ws);
}
```

```cpp
#include <hip/hip_runtime.h>
#include <hip/hip_bf16.h>

typedef unsigned u32;
typedef unsigned long long u64;
typedef u32 v4u __attribute__((ext_vector_type(4)));
typedef u32 v2u __attribute__((ext_vector_type(2)));
typedef short s16x4 __attribute__((ext_vector_type(4)));
typedef short s16x8 __attribute__((ext_vector_type(8)));
typedef __bf16 bf16x2 __attribute__((ext_vector_type(2)));
typedef __bf16 bf16x8 __attribute__((ext_vector_type(8)));
typedef float f32x2 __attribute__((ext_vector_type(2)));
typedef float f32x4 __attribute__((ext_vector_type(4)));
typedef float f32x16 __attribute__((ext_vector_type(16)));
#define GAS __attribute__((address_space(1)))
#define LAS __attribute__((address_space(3)))
#if defined(__HIP_DEVICE_COMPILE__)
#define NOPK __attribute__((target("no-packed-fp32-ops")))
#else
#define NOPK
#endif
#define DI __device__ __forceinline__ NOPK
typedef __amdgpu_buffer_rsrc_t rsrc_t;
template <int V>
struct IC {
  static constexpr int v = V;
};
template <int A, int B, class F>
DI void sfor(F&& f) {
  if constexpr (B - A == 1) {
    f(IC<A>{});
  } else if constexpr (B - A > 1) {
    sfor<A, (A + B) / 2>(f);
    sfor<(A + B) / 2, B>(f);
  }
}
#define LAMBDA __attribute__((always_inline)) NOPK

#define XB_DELAY 0
#define STAMP 0

constexpr u32 WS_FLAG = 4096;
constexpr u32 WS_O = 1024 * 1024;
constexpr u32 WS_H = WS_O + 2 * 1024 * 1024;
constexpr u32 WS_ST = WS_H + 64 * 28 * 1024;
constexpr u32 WS_HH = WS_ST + 256 * 1024;
constexpr u32 WS_END = WS_HH + 64 * 112 * 1024;
constexpr u32 WS_DBG = 16 * 1024 * 1024;
constexpr int FL1 = 0, FL2 = 2048, FL3 = 4096;

constexpr u32 W1M = 448 * 64;
constexpr u32 PL = 14368;
constexpr u32 L_XB = 6 * PL;
constexpr u32 XROW = 928, XBLK = 16 * XROW;
constexpr u32 L_XH = L_XB + 4 * XBLK;
constexpr u32 L_TCH = L_XH + 6144;
constexpr u32 L_EX = L_XH + 8 * XROW;
constexpr u32 L_CS1 = L_EX + 5 * 2048;
constexpr u32 L_PRG = L_CS1 + 384;
constexpr u32 W2PL = 3616, W2Q = 4 * W2PL;
constexpr u32 L_S4 = 8 * PL;
constexpr u32 L_CS3 = L_S4 + 40960;
constexpr u32 PROW = 272;
constexpr u32 LDS_BYTES = 163840;
constexpr int NA4 = 20, NB4 = 8;

#define WAITV_(n) asm volatile("s_waitcnt vmcnt(" #n ")" ::: "memory")
#define WAITV(n) WAITV_(n)
#define BAR() asm volatile("s_waitcnt lgkmcnt(0)\n\ts_barrier" ::: "memory")
#define SB() __builtin_amdgcn_sched_barrier(0)

DI void dma16(u32 lds, u32 voff, rsrc_t rs, u32 soff) {
  asm volatile("s_mov_b32 m0, %0\n\ts_nop 0\n\tbuffer_load_dwordx4 %1, %2, %3 offen lds" ::"s"(lds), "v"(voff), "s"(rs), "s"(soff)
               : "memory");
}
template <int N, int DL, int DS>
DI void dma_run(u32 lds, u32 voff, rsrc_t rs, u32 soff) {
  u32 t;
  asm volatile(
      "s_mov_b32 m0, %1\n\ts_mov_b32 %0, %4\n\t"
      ".rept %c5\n\tbuffer_load_dwordx4 %2, %3, %0 offen lds\n\ts_add_u32 m0, m0, %c6\n\ts_add_u32 %0, %0, %c7\n\t.endr"
      : "=&s"(t)
      : "s"(lds), "v"(voff), "s"(rs), "s"(soff), "i"(N), "i"(DL), "i"(DS)
      : "memory", "scc");
}
template <int N>
DI void dma_run_s(u32 lds, u32 voff, rsrc_t rs, u32 soff, u32 dl, u32 ds) {
  u32 t;
  asm volatile(
      "s_mov_b32 m0, %1\n\ts_mov_b32 %0, %4\n\t"
      ".rept %c5\n\tbuffer_load_dwordx4 %2, %3, %0 offen lds\n\ts_add_u32 m0, m0, %6\n\ts_add_u32 %0, %0, %7\n\t.endr"
      : "=&s"(t)
      : "s"(lds), "v"(voff), "s"(rs), "s"(soff), "i"(N), "s"(dl), "s"(ds)
      : "memory", "scc");
}
template <int N>
DI void waitv() { asm volatile("s_waitcnt vmcnt(%c0)" ::"i"(N) : "memory"); }
DI void st128(v4u v, rsrc_t rs, u32 voff, u32 soff) {
  __builtin_amdgcn_raw_buffer_store_b128(v, rs, voff, soff, 16);
  asm volatile("s_nop 1" ::"v"(v) : "memory");
}
DI rsrc_t mkrs(const void* p, u32 bytes) { return __builtin_amdgcn_make_buffer_rsrc((void*)p, (short)0, (int)bytes, 0x00020000); }
DI v4u trfrag(LAS char* a, LAS char* b) {
  s16x4 lo = __builtin_amdgcn_ds_read_tr16_b64_v4i16((LAS s16x4*)a);
  s16x4 hi = __builtin_amdgcn_ds_read_tr16_b64_v4i16((LAS s16x4*)b);
  return __builtin_bit_cast(v4u, __builtin_shufflevector(lo, hi, 0, 1, 2, 3, 4, 5, 6, 7));
}
DI f32x4 mfma16(v4u a, v4u b, f32x4 c) {
  return __builtin_amdgcn_mfma_f32_16x16x32_bf16(__builtin_bit_cast(bf16x8, a), __builtin_bit_cast(bf16x8, b), c, 0, 0, 0);
}
DI f32x16 mfma32(v4u a, v4u b, f32x16 c) {
  return __builtin_amdgcn_mfma_f32_32x32x16_bf16(__builtin_bit_cast(bf16x8, a), __builtin_bit_cast(bf16x8, b), c, 0, 0, 0);
}
DI float u2f(u32 u) { return __builtin_bit_cast(float, u); }
DI u32 f2u(float f) { return __builtin_bit_cast(u32, f); }
DI u32 pk(float a, float b) {
  f32x2 f = {a, b};
  return __builtin_bit_cast(u32, __builtin_convertvector(f, bf16x2));
}
DI float bflo(u32 u) { return __builtin_bit_cast(float, u << 16); }
DI float bfhi(u32 u) { return __builtin_bit_cast(float, u & 0xffff0000u); }
DI float dot2(u32 a, u32 b, float c) {
  return __builtin_amdgcn_fdot2_f32_bf16(__builtin_bit_cast(bf16x2, a), __builtin_bit_cast(bf16x2, b), c, false);
}
DI float sum16(float x) {
  auto r = __builtin_amdgcn_permlane16_swap(__builtin_bit_cast(u32, x), __builtin_bit_cast(u32, x), false, false);
  const u32 a = r[0], c = r[1];
  return u2f(a) + u2f(c);
}
DI float sum32(float x) {
  auto r = __builtin_amdgcn_permlane32_swap(__builtin_bit_cast(u32, x), __builtin_bit_cast(u32, x), false, false);
  const u32 a = r[0], c = r[1];
  return u2f(a) + u2f(c);
}
DI float max16(float x) {
  auto r = __builtin_amdgcn_permlane16_swap(__builtin_bit_cast(u32, x), __builtin_bit_cast(u32, x), false, false);
  const u32 a = r[0], c = r[1];
  return __builtin_fmaxf(u2f(a), u2f(c));
}
DI float max32(float x) {
  auto r = __builtin_amdgcn_permlane32_swap(__builtin_bit_cast(u32, x), __builtin_bit_cast(u32, x), false, false);
  const u32 a = r[0], c = r[1];
  return __builtin_fmaxf(u2f(a), u2f(c));
}
DI float bperm(int srclane, float x) {
  return __builtin_bit_cast(float, __builtin_amdgcn_ds_bpermute(srclane * 4, __builtin_bit_cast(int, x)));
}
#define RLX_AGENT __ATOMIC_RELAXED, __HIP_MEMORY_SCOPE_AGENT
#define RLX_WG __ATOMIC_RELAXED, __HIP_MEMORY_SCOPE_WORKGROUP
#define WAITL() asm volatile("s_waitcnt lgkmcnt(0)" ::: "memory")
DI void poll(char* ws, int idx, bool act, u64 tag) {
  GAS u64* p = (GAS u64*)(ws + WS_FLAG + (u32)idx * 128u);
#pragma unroll 1
  for (int spins = 0; spins < (1 << 16); ++spins) {
    u64 v = tag;
    if (act) v = __hip_atomic_load(p, RLX_AGENT);
    if (__builtin_amdgcn_ballot_w64(v != tag) == 0) break;
    __builtin_amdgcn_s_sleep(2);
  }
  asm volatile("" ::: "memory");
}
DI void setflag(char* ws, int idx, u64 tag, int lane) {
  if (lane == 0) __hip_atomic_store((GAS u64*)(ws + WS_FLAG + (u32)idx * 128u), tag, RLX_AGENT);
}
DI void prog(LAS char* L, int w, u32 stage) {
  asm volatile("" ::: "memory");
  *(volatile LAS u32*)(L + L_PRG + w * 4) = stage;
  SB();
}
DI void await(LAS char* L, u32 need, int lane) {
  const u32 nd = (need >> (4 * (lane & 7))) & 15u;
#pragma unroll 1
  for (int spins = 0; spins < (1 << 20); ++spins) {
    const u32 v = *(volatile LAS u32*)(L + L_PRG + (lane & 7) * 4);
    if (__builtin_amdgcn_ballot_w64(v < nd) == 0) break;
    __builtin_amdgcn_s_sleep(1);
  }
  asm volatile("" ::: "memory");
}
DI void touch(u32 lds, rsrc_t rs, u32 voff, u32 soff) {
  asm volatile("s_mov_b32 m0, %0\n\ts_nop 0\n\tbuffer_load_dword %1, %2, %3 offen sc1 lds" ::"s"(lds), "v"(voff), "s"(rs), "s"(soff)
               : "memory");
}
#define TS(i)
#define TC(i)
#define TSOUT()

template <int N>
DI void stage_w4(v4u (&w4f)[NA4][2], LAS char* L, u32 L0, rsrc_t rsWd, u32 so, int w, int lane) {
  const u32 vo = (lane >> 3) * 896 + (((lane & 7) ^ ((lane >> 2) & 4)) * 16);
  const u32 lb = L_S4 + (w & 3) * 10240;
  const int tq = (lane >> 2) & 3;
  LAS char* a0 = L + lb + (4 * (lane >> 5) + tq) * 128 + ((tq >> 1) * 64) + ((lane >> 4) & 1) * 32 + (lane & 3) * 8;
  LAS char* a1 = L + lb + (4 * (lane >> 5) + tq) * 128 + (((tq >> 1) ^ 1) * 64) + ((lane >> 4) & 1) * 32 + (lane & 3) * 8;
  sfor<0, N>([&](auto K_) LAMBDA {
    constexpr int k = decltype(K_)::v;
    constexpr int last = (k + 3 > 4 ? k + 3 : 4) < N - 1 ? (k + 3 > 4 ? k + 3 : 4) : N - 1;
    waitv<2 * (last - k)>();
    w4f[k][0] = trfrag(a0 + (k % 5) * 2048, a0 + (k % 5) * 2048 + 1024);
    w4f[k][1] = trfrag(a1 + (k % 5) * 2048, a1 + (k % 5) * 2048 + 1024);
    if constexpr (k >= 1 && k + 4 < N) {
      asm volatile("s_waitcnt lgkmcnt(4)" ::: "memory");
      dma_run<2, 1024, 8 * 896>(L0 + lb + ((k - 1) % 5) * 2048, vo, rsWd, so + (k + 4) * 16 * 896);
    }
  });
}
template <int N>
DI void phase4(v4u (&w4f)[NA4][2], LAS char* L, char* ws, rsrc_t rsWS, int k0, int t32a, u64 tag, int w, int lane, u32& stampv) {
  const int hh = lane >> 5;
#pragma unroll 1
  for (int st = 0; st < 2; ++st) {
    const int t32 = t32a + 4 * st;
    poll(ws, FL3 + t32 * 28 + (k0 >> 2) + lane, lane < N / 4, tag);
    TS(29 + 4 * st);
    constexpr int D = N < 10 ? N : 10;
    v4u hf[D];
#pragma unroll
    for (int kk = 0; kk < D; ++kk) hf[kk] = __builtin_amdgcn_raw_buffer_load_b128(rsWS, lane * 16, WS_HH + (t32 * 112 + k0 + kk) * 1024, 16);
    SB();
    f32x16 acc[2];
#pragma unroll
    for (int ct = 0; ct < 2; ++ct)
#pragma unroll
      for (int i = 0; i < 16; ++i) acc[ct][i] = 0.f;
#pragma unroll
    for (int kk = 0; kk < N; ++kk) {
      const v4u cur = hf[kk % D];
      acc[0] = mfma32(w4f[kk][0], cur, acc[0]);
      acc[1] = mfma32(w4f[kk][1], cur, acc[1]);
      if (kk + D < N) hf[kk % D] = __builtin_amdgcn_raw_buffer_load_b128(rsWS, lane * 16, WS_HH + (t32 * 112 + k0 + kk + D) * 1024, 16);
      SB();
    }
    TS(30 + 4 * st);
    if (st == 0) BAR();
#pragma unroll
    for (int ct = 0; ct < 2; ++ct)
#pragma unroll
      for (int i = 0; i < 4; ++i)
        *(LAS f32x4*)(L + ((st * 8 + w) * 32 + (lane & 31)) * PROW + (32 * ct + 8 * i + 4 * hh) * 4) =
            f32x4{acc[ct][4 * i], acc[ct][4 * i + 1], acc[ct][4 * i + 2], acc[ct][4 * i + 3]};
    TS(31 + 4 * st);
  }
}
DI v2u load_epoch(rsrc_t rs) {
  v2u e;
  asm volatile("buffer_load_dwordx2 %0, off, %1, 0 sc1\n\ts_waitcnt vmcnt(0)" : "=&v"(e) : "s"(rs) : "memory");
  return e;
}
DI float bcast16(float x) {
  return u2f(__builtin_amdgcn_update_dpp(0, (int)f2u(x), 0x150, 0xf, 0xf, true));
}

NOPK __global__ __launch_bounds__(512, 2) void fused_block(const void* __restrict__ xg, const void* __restrict__ wqkv,
                                                      const void* __restrict__ wo, const void* __restrict__ wg,
                                                      const void* __restrict__ wu, const void* __restrict__ wd,
                                                      void* __restrict__ outg, char* ws) {
  __shared__ __attribute__((aligned(16))) char smem[LDS_BYTES];
  LAS char* L = (LAS char*)smem;
  const u32 L0 = (u32)(size_t)L;
  const u32 tid = __builtin_amdgcn_workitem_id_x();
  const int b = __builtin_amdgcn_workgroup_id_x(), xcd = b & 7, jj = b >> 3;
  const int lane = tid & 63, w = __builtin_amdgcn_readfirstlane(tid >> 6);
  const int r16 = lane & 15, g = lane >> 4, tq = (lane >> 2) & 3, tp = lane & 3, hh = lane >> 5;
  u32 stampv = 0;
  TS(0);
  TC(48);
  u64 pc;
  u32 clen;
  asm volatile("s_getpc_b64 %0\n.Lfb_here:\n\ts_mov_b32 %1, .Lfb_last-.Lfb_here" : "=s"(pc), "=s"(clen));
  if (tid < 240) *(LAS u32*)(L + (tid < 224 ? L_XH : L_PRG - 896) + tid * 4) = tid < 224 ? 0x3f803f80u : 0u;

  const int tile1 = (xcd >> 1) * 4 + (jj & 3), head = (xcd & 1) * 8 + (jj >> 2);
  const int srow0 = 256 * (tile1 >> 1) + (tile1 & 1);
  const u32 vo1 = (lane >> 2) * 2688 + ((lane & 3) ^ (2 * hh)) * 16;
  const u32 so1 = (w - 5) * 896 + ((56 * head) & ~15), l1 = L0 + (w - 5) * W1M;
  const u32 f_l = w < 4 ? L0 + L_XB + w * XBLK : l1, f_v = w < 4 ? lane * 16 : vo1, f_s = w < 4 ? (u32)((srow0 + 32 * w) * 896) : so1;
  const u32 f_dl = w < 4 ? XROW : 1024, f_ds = w < 4 ? 1792 : 16 * 2688;
  asm volatile("" ::"s"(f_l), "v"(f_v), "s"(f_s), "s"(f_dl), "s"(f_ds));
  asm volatile("" ::"s"(xg), "s"(wqkv), "s"(wo), "s"(wg), "s"(wu), "s"(wd), "s"(outg), "s"(ws));
  BAR();
  const rsrc_t rsWS = mkrs(ws, WS_END), rsX = mkrs(xg, 2048 * 896), rsW1 = mkrs(wqkv, 448 * 2688);
  if (w < 4) __builtin_amdgcn_s_setprio(2);
  if (w != 4) {
    if (w > 4 || lane < 56) dma_run_s<16>(f_l, f_v, w < 4 ? rsX : rsW1, f_s, f_dl, f_ds);
    if (w < 4 && lane < 56) {
      const int hr = w == 0 ? 1 : 2 * w;
      dma_run<2, XROW, 1792>(L0 + L_XH + hr * XROW, lane * 16, rsX, (tile1 < 2 ? 0 : srow0 - 16 + 2 * hr) * 896);
    }
  } else {
    if (lane < 8) touch(L0 + L_PRG + 64, mkrs((const void*)pc, clen), jj * 1024 + lane * 128, 0);
  }
  const rsrc_t rsWo = mkrs(wo, 448 * 896), rsWg = mkrs(wg, 448 * 3584), rsWu = mkrs(wu, 448 * 3584), rsWd = mkrs(wd, 1792 * 896);
  const bool tail = jj < 28;
  const int tt2 = jj & 3, c2 = jj >> 2;
  const int T3 = (xcd >> 2) * 4 + (jj & 3), fs3 = (xcd & 3) * 7 + (jj >> 2);
  const int stw = w >> 2;
  const u32 vo3 = (lane >> 3) * 4 * 3584 + (lane & 7) * 16;
  {
    v4u xf[14];
    float nmean = 0.f, rstd = 0.f;
    auto rdx = [&](LAS char* xa) LAMBDA {
#pragma unroll
      for (int ks = 0; ks < 14; ++ks) xf[ks] = *(LAS v4u*)(xa + ks * 64);
    };
    auto stats = [&]() LAMBDA {
      float s1[4] = {0.f, 0.f, 0.f, 0.f}, s2[4] = {0.f, 0.f, 0.f, 0.f};
#pragma unroll
      for (int ks = 0; ks < 14; ++ks)
#pragma unroll
        for (int e = 0; e < 4; ++e) {
          s1[e] = dot2(xf[ks][e], 0x3f803f80u, s1[e]);
          s2[e] = dot2(xf[ks][e], xf[ks][e], s2[e]);
        }
      const float t1 = sum32(sum16((s1[0] + s1[1]) + (s1[2] + s1[3]))), t2 = sum32(sum16((s2[0] + s2[1]) + (s2[2] + s2[3])));
      nmean = t1 * (-1.f / 448.f);
      rstd = __builtin_amdgcn_rsqf(__builtin_fmaxf(t2 * (1.f / 448.f) - nmean * nmean, 0.f) + 1e-5f);
      asm volatile("" : "+v"(nmean), "+v"(rstd));
    };
    LAS char* wa[4];
#pragma unroll
    for (int i = 0; i < 4; ++i) {
      wa[i] = L + (i >> 1) * 2 * W1M + (8 * g + tq) * 64 + (((4 * (i & 1) + tp + (head & 1)) & 7) ^ (4 * (g & 1))) * 8;
      asm volatile("" : "+v"(wa[i]));
    }
    LAS char* xh = L + L_XH + (r16 & 7) * XROW + g * 16;
    if (w <= 4) {
      WAITV(0);
      if (w < 4) prog(L, w, 1);
      else await(L, 0x00001111u, lane);
      TS(2);
      rdx(w < 4 ? L + L_XB + w * XBLK + r16 * XROW + g * 16 : xh);
      if (w < 4) prog(L, w, 2);
      stats();
    }
    if (w == 4) {
      *(LAS f32x2*)(L + L_PRG + 64 + r16 * 8) = f32x2{nmean, rstd};
      prog(L, w, 1);
      await(L, 0x33302222u, lane);
      if (XB_DELAY && !((0x2A54 >> head) & 1)) __builtin_amdgcn_s_sleep(XB_DELAY);
      if (lane < 56) {
#pragma unroll 1
        for (int k = 0; k < 4; ++k) dma_run<16, XROW, 1792>(L0 + L_XB + k * XBLK, lane * 16, rsX, (srow0 + 128 + 32 * k) * 896);
      }
      const u32 tv = lane < 49 ? b * 6272 + lane * 128 : 0x7fff0000u;
      touch(L0 + L_TCH, rsWo, lane < 13 ? b * 1664 + lane * 128 : 0x7fff0000u, 0);
      touch(L0 + L_TCH, rsWg, tv, 0);
      touch(L0 + L_TCH, rsWu, tv, 0);
      touch(L0 + L_TCH, rsWd, tv, 0);
      WAITV(4);
      prog(L, w, 6);
    } else if (w > 4) {
      WAITV(8);
      prog(L, w, 1);
      dma_run<12, 1024, 16 * 2688>(l1 + 16 * 1024, vo1, rsW1, so1 + 256 * 2688);
      __builtin_amdgcn_s_setprio(3);
      await(L, 0x00001111u, lane);
      rdx(xh);
      f32x4 h0 = {0.f, 0.f, 0.f, 0.f}, h1 = {0.f, 0.f, 0.f, 0.f};
      LAS char *a0 = wa[0] + (w - 5) * W1M, *a1 = wa[1] + (w - 5) * W1M;
      auto part = [&](auto A_, auto B_) LAMBDA {
        constexpr int A = decltype(A_)::v, B = decltype(B_)::v;
        v4u f[B - A][2];
#pragma unroll
        for (int ks = A; ks < B; ++ks) {
          f[ks - A][0] = trfrag(a0 + ks * 2048, a0 + ks * 2048 + 256);
          f[ks - A][1] = trfrag(a1 + ks * 2048, a1 + ks * 2048 + 256);
        }
        SB();
#pragma unroll
        for (int ks = A; ks < B; ++ks) {
          h0 = mfma16(f[ks - A][0], xf[ks], h0);
          h1 = mfma16(f[ks - A][1], xf[ks], h1);
        }
        SB();
      };
      WAITV(12);
      prog(L, w, 2);
      part(IC<0>{}, IC<8>{});
      WAITV(4);
      prog(L, w, 3);
      part(IC<8>{}, IC<12>{});
      await(L, 0x00010000u, lane);
      const f32x2 hs = *(LAS f32x2*)(L + L_PRG + 64 + r16 * 8);
      WAITV(0);
      prog(L, w, 4);
      part(IC<12>{}, IC<14>{});
      if (r16 == 0) {
        *(LAS f32x4*)(L + L_CS1 + ((w - 5) * 32 + 4 * g) * 4) = h0;
        *(LAS f32x4*)(L + L_CS1 + ((w - 5) * 32 + 16 + 4 * g) * 4) = h1;
      }
      prog(L, w, 5);
      float t0[4], t1[4];
#pragma unroll
      for (int j = 0; j < 4; ++j) {
        t0[j] = (h0[j] + bcast16(h0[j]) * hs[0]) * hs[1];
        t1[j] = (h1[j] + bcast16(h1[j]) * hs[0]) * hs[1];
      }
      const v2u lo = {pk(t0[0], t0[1]), pk(t0[2], t0[3])}, hi = {g == 3 ? 0u : pk(t1[0], t1[1]), g == 3 ? 0u : pk(t1[2], t1[3])};
      if (w != 5) {
        *(LAS v2u*)(L + L_EX + (w == 6 ? lane * 16 : 1024 + r16 * 64 + g * 8)) = lo;
        *(LAS v2u*)(L + L_EX + (w == 6 ? lane * 16 + 8 : 1024 + r16 * 64 + g * 8 + 32)) = hi;
      }
      prog(L, w, 6);
      __builtin_amdgcn_s_setprio(0);
    }
    if (w >= 4) {
      await(L, 0x66660000u, lane);
      TS(2);
      rdx(L + L_XB + (w & 3) * XBLK + r16 * XROW + g * 16);
      prog(L, w, 7);
    } else {
      await(L, 0x11100000u, lane);
    }
    TS(7);
    f32x4 acc[6];
#pragma unroll
    for (int i = 0; i < 6; ++i) acc[i] = f32x4{0.f, 0.f, 0.f, 0.f};
    constexpr int PD = 5;
    v4u wf[PD + 1];
    auto rdw = [&](int n) LAMBDA {
      const int i = n % 6;
      LAS char* a = (i < 4 ? wa[i & 1] + (i >> 1) * W1M : wa[2 + (i & 1)]) + (n / 6) * 2048;
      return trfrag(a, a + 256);
    };
#pragma unroll
    for (int n = 0; n < PD; ++n) wf[n] = rdw(n);
    SB();
#pragma unroll
    for (int n = 0; n < 84; ++n) {
      if ((n + PD) % 24 == 0 && w < 4) await(L, 0x11100000u * ((n + PD) / 24 + 1), lane);
      if (n + PD < 84) wf[(n + PD) % (PD + 1)] = rdw(n + PD);
      acc[n % 6] = mfma16(wf[n % (PD + 1)], xf[n / 6], acc[n % 6]);
      SB();
    }
    TS(3);
    if (w >= 4) {
      prog(L, w, 8);
      stats();
      if (tail) {
        await(L, 0x88883333u, lane);
        dma_run<7, 1024, 32 * 3584>(L0 + (w - 4) * PL, vo3, rsWg, (w - 4) * 3584 + fs3 * 128);
      }
    }
    f32x4 cs[6];
#pragma unroll 1
    for (int spins = 0; spins < (1 << 20); ++spins) {
      const u32 f = *(volatile LAS u32*)(L + L_PRG + (lane & 7) * 4);
#pragma unroll
      for (int i = 0; i < 6; ++i) cs[i] = *(volatile LAS f32x4*)(L + L_CS1 + (16 * i + 4 * g) * 4);
      if (__builtin_amdgcn_ballot_w64((lane & 7) >= 5 && f < 5) == 0) break;
      __builtin_amdgcn_s_sleep(1);
    }
    auto nrm = [&](int i, float r) LAMBDA {
      v2u o = {pk((acc[i][0] + nmean * cs[i][0]) * r, (acc[i][1] + nmean * cs[i][1]) * r),
               pk((acc[i][2] + nmean * cs[i][2]) * r, (acc[i][3] + nmean * cs[i][3]) * r)};
      if (i & 1) o = g == 3 ? v2u{0u, 0u} : o;
      return o;
    };
    const u32 kw = w < 4 ? L_EX + (w + 1) * 2048 : L_XH + (w - 4) * 2048, vw = w == 7 ? L_XH + 6400 : kw + 1024;
    const u32 kr = w <= 4 ? L_EX + w * 2048 : L_XH + (w - 5) * 2048;
    const v2u k0 = nrm(2, rstd), k1 = nrm(3, rstd);
    const v4u kf = {k0[0], k0[1], k1[0], k1[1]};
    if (w != 7) *(LAS v4u*)(L + kw + lane * 16) = kf;
    *(LAS v2u*)(L + vw + r16 * 64 + g * 8) = nrm(4, rstd);
    *(LAS v2u*)(L + vw + r16 * 64 + g * 8 + 32) = nrm(5, rstd);
    prog(L, w, w < 4 ? 3 : 9);
    const float rq = rstd * (0.18898223650461362f * 1.4426950408889634f);
    const v2u q0 = nrm(0, rq), q1 = nrm(1, rq);
    const v4u qf = {q0[0], q0[1], q1[0], q1[1]};
    const f32x4 z = {0.f, 0.f, 0.f, 0.f};
    const f32x4 so = mfma16(kf, qf, z);
    if (w >= 4 && tail) dma_run<7, 1024, 32 * 3584>(L0 + (w - 4) * PL + 7168, vo3, rsWg, (w - 4 + 224) * 3584 + fs3 * 128);
    const u32 pneed = w == 0 ? 0x66000000u : (w <= 4 ? 3u : 9u) << (4 * (w - 1));
    const u32 nd = (pneed >> (4 * (lane & 7))) & 15u;
    const u32 vt = (4 * g + tq) * 64 + tp * 8;
    v4u kp, v0, v1;
#pragma unroll 1
    for (int spins = 0; spins < (1 << 20); ++spins) {
      const u32 f = *(volatile LAS u32*)(L + L_PRG + (lane & 7) * 4);
      asm volatile("" ::: "memory");
      kp = *(LAS v4u*)(L + kr + lane * 16);
      v0 = trfrag(L + kr + 1024 + vt, L + vw + vt);
      v1 = trfrag(L + kr + 1024 + vt + 32, L + vw + vt + 32);
      asm volatile("" ::: "memory");
      if (__builtin_amdgcn_ballot_w64(f < nd) == 0) break;
      __builtin_amdgcn_s_sleep(1);
    }
    TS(4);
    const f32x4 sp = mfma16(kp, qf, z);
    const bool noprev = (tile1 < 2) && (w == 0);
    float p[8];
    float mx = -3.0e38f;
#pragma unroll
    for (int j = 0; j < 4; ++j) {
      const int d = r16 - (4 * g + j);
      p[j] = (d <= -9 && !noprev) ? sp[j] : -3.0e38f;
      p[4 + j] = (d >= 0 && d <= 7) ? so[j] : -3.0e38f;
      mx = __builtin_fmaxf(mx, __builtin_fmaxf(p[j], p[4 + j]));
    }
    mx = max32(max16(mx));
    float sum = 0.f;
#pragma unroll
    for (int j = 0; j < 8; ++j) {
      p[j] = __builtin_amdgcn_exp2f(p[j] - mx);
      sum += p[j];
    }
    const v4u pf = {pk(p[0], p[1]), pk(p[2], p[3]), pk(p[4], p[5]), pk(p[6], p[7])};
    const f32x4 o0 = mfma16(v0, pf, z), o1 = mfma16(v1, pf, z);
    sum = sum32(sum16(sum));
    const float inv = __builtin_amdgcn_rcpf(sum);
    const v4u of = {pk(o0[0] * inv, o0[1] * inv), pk(o0[2] * inv, o0[3] * inv), pk(o1[0] * inv, o1[1] * inv), pk(o1[2] * inv, o1[3] * inv)};
    st128(of, rsWS, lane * 16, WS_O + ((tile1 * 8 + w) * 16 + head) * 1024);
    TS(5);
  }
  u64 tag;
  {
    const v2u epoch = load_epoch(rsWS);
    const u32 lo = __builtin_amdgcn_readfirstlane(epoch[0]), hi = __builtin_amdgcn_readfirstlane(epoch[1]);
    tag = (((u64)hi << 32) | lo) * 6364136223846793005ull + 1442695040888963407ull;
  }
  setflag(ws, FL1 + (tile1 * 8 + w) * 16 + head, tag, lane);
  TS(6);
  if (!tail) {
    if (w < 4) {
#pragma unroll 1
      for (int k = 0; k < 7; ++k) {
        const u32 r = ((k * 4 + (jj - 28)) * 2 + (w >> 1)) * 8 + (lane >> 3);
        touch(L0 + L_TCH, (w & 1) ? rsWu : rsWg, (lane & 7) < 7 ? r * 3584 + ((xcd & 3) * 7 + (lane & 7)) * 128 : 0x7fff0000u, 0);
      }
    }
#pragma unroll 1
    for (int k = 0; k < 7; ++k) {
      const u32 idx = ((k * 4 + (jj - 28)) * 8 + w) * 64 + lane;
      touch(L0 + L_TCH, rsWd, idx < 12544 ? (idx < 3584 ? idx + 8960 : idx - 3584) * 128 : 0x7fff0000u, 0);
    }
    if (b == 255) {
#pragma unroll 1
      for (int k = 0; k < 4; ++k) poll(ws, FL1 + (w * 4 + k) * 64 + lane, true, tag);
      if (lane == 0) {
        u32 old = __hip_atomic_fetch_add((LAS u32*)(L + L_PRG + 32), 1u, RLX_WG);
        if (old == 7) __hip_atomic_store((GAS u64*)ws, tag, RLX_AGENT);
      }
    }
    TSOUT();
    return;
  }

  const int p2 = tt2 >> 1, q2 = tt2 & 1, rb = w & 1, ch2 = (w >> 1) & 1;
  const u32 frow = (256 * xcd + 2 * (64 * stw + 32 * q2 + 16 * rb + r16) + p2) * 896 + c2 * 128 + ch2 * 64 + g * 8;
  const int k04 = w < 4 ? NA4 * w : 4 * NA4 + NB4 * (w - 4);
  const u32 so4 = 16 * k04 * 896 + c2 * 128;
  v4u hres;
  if (w >= 4) __builtin_amdgcn_s_setprio(3);
  {
    const v2u xa = __builtin_amdgcn_raw_buffer_load_b64(rsX, frow, 0, 0), xb = __builtin_amdgcn_raw_buffer_load_b64(rsX, frow, 32, 0);
    SB();
    const int blk = (xcd * 2 + p2) * 8 + 4 * stw + 2 * q2 + rb;
    v4u of[16], w2f[16][2];
    auto load_o = [&]() __attribute__((always_inline)) NOPK {
#pragma unroll
      for (int hd = 0; hd < 16; ++hd) of[hd] = __builtin_amdgcn_raw_buffer_load_b128(rsWS, lane * 16, WS_O + (blk * 16 + hd) * 1024, 16);
      SB();
    };
    if (w < 4) {
      const u32 vo2 = (lane >> 3) * 4 * 896 + (lane & 7) * 16;
      await(L, 0x77770000u, lane);
      const u32 l2 = L0 + L_XB + w * W2Q, s2 = 112 * w * 896 + c2 * 128;
      dma_run<4, W2PL, 896>(l2, vo2, rsWo, s2);
      dma_run<4, W2PL, 896>(l2 + 1024, vo2, rsWo, s2 + 32 * 896);
      dma_run<4, W2PL, 896>(l2 + 2048, vo2, rsWo, s2 + 64 * 896);
      if (lane < 32) dma_run<4, W2PL, 896>(l2 + 3072, vo2, rsWo, s2 + 96 * 896);
      TS(16);
      WAITV(0);
      prog(L, w, 4);
      poll(ws, FL1 + blk * 16 + lane, lane < 16, tag);
      TS(17);
      load_o();
    } else {
      dma_run<7, 1024, 32 * 3584>(L0 + (4 + ((w >> 1) & 1)) * PL + (w & 1) * 7168, vo3, rsWu, (((w >> 1) & 1) + (w & 1) * 224) * 3584 + fs3 * 128);
    }
    await(L, 0x00004444u, lane);
    {
      LAS char* a = L + L_XB + tq * W2PL + g * 128 + tp * 8 + ch2 * 64;
      LAS char* a2 = a + (g < 3 ? 512 : 0);
#pragma unroll
      for (int hd = 0; hd < 16; ++hd)
#pragma unroll
        for (int cb = 0; cb < 2; ++cb) {
          const u32 o = (hd >> 2) * W2Q + (hd & 3) * 896 + cb * 32;
          w2f[hd][cb] = trfrag(a + o, a2 + o);
        }
    }
    prog(L, w, w < 4 ? 5 : 10);
    if (w >= 4) {
      await(L, 0xAAAA5555u, lane);
      dma_run<7, 1024, 32 * 3584>(L0 + (6 + ((w >> 1) & 1)) * PL + (w & 1) * 7168, vo3, rsWu,
                                  ((2 + ((w >> 1) & 1)) + (w & 1) * 224) * 3584 + fs3 * 128);
      dma_run<10, 1024, 8 * 896>(L0 + L_S4 + (w & 3) * 10240, (lane >> 3) * 896 + (((lane & 7) ^ ((lane >> 2) & 4)) * 16), rsWd, so4);
      WAITV(10);
      prog(L, w, 11);
      await(L, 0xBBBB0000u, lane);
      {
        f32x4 cg = {0.f, 0.f, 0.f, 0.f}, cu = {0.f, 0.f, 0.f, 0.f};
        const v4u ones = {0x3f803f80u, 0x3f803f80u, 0x3f803f80u, 0x3f803f80u};
        LAS char* ag = L + tq * PL + g * 128 + tp * 8 + (w - 4) * 32;
        LAS char* au = ag + 4 * PL;
        asm volatile("" : "+v"(au));
        v4u f[6];
        sfor<0, 34>([&](auto N_) LAMBDA {
          constexpr int n = decltype(N_)::v;
          if constexpr (n >= 6) {
            if constexpr (n & 1) cu = mfma16(ones, f[n % 6], cu);
            else cg = mfma16(ones, f[n % 6], cg);
          }
          if constexpr (n < 28) {
            LAS char* a = ((n & 1) ? au : ag) + (n >> 1) * 1024;
            f[n % 6] = trfrag(a, a + 512);
          }
          SB();
        });
        const float c = (g & 1) ? cu[0] : cg[0];
        const u32 hi = pk(c, c);
        const u32 d0 = pk(bflo(hi), c - bflo(hi));
        *(LAS v4u*)(L + L_CS3 + (w - 4) * 1024 + lane * 16) = v4u{hh ? 0u : d0, hh ? 0u : (hi & 0xffffu), 0u, 0u};
        prog(L, w, 12);
      }
      TS(16);
      poll(ws, FL1 + blk * 16 + lane, lane < 16, tag);
      TS(17);
      load_o();
    }
    f32x4 a0 = {bflo(xa[0]), bfhi(xa[0]), bflo(xa[1]), bfhi(xa[1])}, a1 = {bflo(xb[0]), bfhi(xb[0]), bflo(xb[1]), bfhi(xb[1])};
#pragma unroll
    for (int hd = 0; hd < 16; ++hd) {
      a0 = mfma16(w2f[hd][0], of[hd], a0);
      a1 = mfma16(w2f[hd][1], of[hd], a1);
    }
    TS(18);
    hres = v4u{pk(a0[0], a0[1]), pk(a0[2], a0[3]), pk(a1[0], a1[1]), pk(a1[2], a1[3])};
    const int t32 = (xcd * 2 + stw) * 4 + tt2;
    st128(hres, rsWS, (g >> 1) * 1024 + ((g & 1) * 32 + 16 * rb + r16) * 16,
                                           WS_H + (t32 * 28 + 4 * c2 + 2 * ch2) * 1024);
    float s1 = 0.f, s2 = 0.f;
#pragma unroll
    for (int i = 0; i < 4; ++i) {
      const float lo = bflo(hres[i]), hi = bfhi(hres[i]);
      s1 += lo + hi;
      s2 += lo * lo + hi * hi;
    }
    s1 = sum32(sum16(s1));
    s2 = sum32(sum16(s2));
    if (lane < 16)
      __builtin_amdgcn_raw_buffer_store_b64(v2u{f2u(s1), f2u(s2)}, rsWS, (16 * rb + r16) * 8,
                                            WS_ST + (t32 * 14 + 2 * c2 + ch2) * 256, 16);
    TS(19);
    WAITV(0);
    setflag(ws, FL2 + t32 * 28 + c2 * 4 + (w & 3), tag, lane);
    TS(20);
  }

  v4u w4f[NA4][2];
  LAS char* a3 = L + (4 * (g & 1) + tq) * PL + hh * 128 + tp * 8;
  if (w < 4) {
    await(L, 0xBBBB0000u, lane);
#pragma unroll
    for (int kk = 0; kk < NB4; ++kk) w4f[kk][0] = w4f[kk][1] = v4u{0u, 0u, 0u, 0u};
  } else {
    stage_w4<NB4>(w4f, L, L0, rsWd, so4, w, lane);
    prog(L, w, 13);
  }
  TS(21);
  {
    const int t32 = (T3 * 2 + stw) * 4 + (w & 3);
    poll(ws, FL2 + t32 * 28 + lane, lane < 28, tag);
    TS(24);
    TC(49);
    constexpr int HD = 14, PD = 4;
    v4u hb[HD];
#pragma unroll
    for (int ks = 0; ks < HD; ++ks) hb[ks] = __builtin_amdgcn_raw_buffer_load_b128(rsWS, lane * 16, WS_H + (t32 * 28 + ks) * 1024, 16);
    v2u sv[7];
#pragma unroll
    for (int i = 0; i < 7; ++i) sv[i] = __builtin_amdgcn_raw_buffer_load_b64(rsWS, lane * 8, WS_ST + (t32 * 14 + 2 * i) * 256, 16);
    if (w >= 4) __builtin_amdgcn_s_setprio(1);
    f32x16 acc[4];
#pragma unroll
    for (int c = 0; c < 4; ++c)
#pragma unroll
      for (int i = 0; i < 16; ++i) acc[c][i] = 0.f;
    v4u wf[PD + 1];
    auto rdw = [&](int n) __attribute__((always_inline)) NOPK {
      const int ks = n < 68 ? n >> 2 : 17 + (n - 68) % 12, c = n < 68 ? n & 3 : (n - 68) / 12;
      if (ks == 28) return *(LAS v4u*)(L + L_CS3 + c * 1024 + lane * 16);
      const u32 o = (ks >> 1) * 1024 + (ks & 1) * 256 + c * 32;
      return trfrag(a3 + o, a3 + o + 512);
    };
#pragma unroll
    for (int n = 0; n < PD; ++n) wf[n] = rdw(n);
    float s1 = 0.f, s2 = 0.f, ng = 0.f, irs = 0.f, o[32];
    v4u mf = {0u, 0u, 0u, 0u};
    SB();
    sfor<0, 134>([&](auto N_) LAMBDA {
      constexpr int n = decltype(N_)::v;
      if (n == 4) TC(50);
      if (n == 66) await(L, 0xCCCC0000u, lane);
      if constexpr (n < 116) {
        constexpr int ks = n < 68 ? n >> 2 : 17 + (n - 68) % 12, c = n < 68 ? n & 3 : (n - 68) / 12;
        if (n + PD < 116) wf[(n + PD) % (PD + 1)] = rdw(n + PD);
        acc[c] = mfma32(wf[n % (PD + 1)], ks == 28 ? mf : hb[ks % HD], acc[c]);
        if (n < 68 && c == 3 && ks + HD < 28)
          hb[ks % HD] = __builtin_amdgcn_raw_buffer_load_b128(rsWS, lane * 16, WS_H + (t32 * 28 + ks + HD) * 1024, 16);
      }
      if constexpr (n >= 50 && n < 57) {
        s1 += u2f(sv[n - 50][0]);
        s2 += u2f(sv[n - 50][1]);
      }
      if (n == 58) {
        s1 = sum32(s1) * (-1.f / 448.f);
        s2 = sum32(s2);
      }
      if (n == 60) {
        const float var = __builtin_fmaxf(s2 * (1.f / 448.f) - s1 * s1, 0.f) + 1e-5f;
        const float rstd = __builtin_amdgcn_rsqf(var);
        ng = -rstd * 1.4426950408889634f;
        irs = var * rstd;
      }
      if (n == 62) {
        const u32 hi = pk(s1, s1);
        mf[0] = hh ? 0u : hi;
        mf[1] = hh ? 0u : pk(s1 - bflo(hi), 0.f);
      }
      sfor<0, 32>([&](auto V_) LAMBDA {
        constexpr int V = decltype(V_)::v, c = V >> 3, i = V & 7, d = n - (81 + 3 * V / 2);
        if constexpr (d == 0) o[V] = acc[c][i] * ng;
        if constexpr (d == 1) o[V] = __builtin_amdgcn_exp2f(o[V]);
        if constexpr (d == 2) o[V] = o[V] * irs + irs;
        if constexpr (d == 3) o[V] = __builtin_amdgcn_rcpf(o[V]);
        if constexpr (d == 4) o[V] *= acc[c][8 + i];
        if constexpr (d == 5 && i == 7)
          st128(v4u{pk(o[V - 7], o[V - 6]), pk(o[V - 5], o[V - 4]), pk(o[V - 3], o[V - 2]), pk(o[V - 1], o[V])}, rsWS, lane * 16,
                WS_HH + (t32 * 112 + fs3 * 4 + c) * 1024);
      });
      if (n < 116) SB();
      if (n == 115) {
        TS(25);
        TC(51);
      }
    });
    TS(26);
    TC(52);
    if (w < 4) {
      await(L, 0xDDDD0000u, lane);
      dma_run<10, 1024, 8 * 896>(L0 + L_S4 + w * 10240, (lane >> 3) * 896 + (((lane & 7) ^ ((lane >> 2) & 4)) * 16), rsWd, so4);
      WAITV(10);
    } else {
      WAITV(0);
    }
    setflag(ws, FL3 + t32 * 28 + fs3, tag, lane);
    TS(27);
  }
  if (w < 4) {
    stage_w4<NA4>(w4f, L, L0, rsWd, so4, w, lane);
    __builtin_amdgcn_s_setprio(0);
    TS(28);
    phase4<NA4>(w4f, L, ws, rsWS, k04, xcd * 8 + tt2, tag, w, lane, stampv);
  } else {
    phase4<NB4>(w4f, L, ws, rsWS, k04, xcd * 8 + tt2, tag, w, lane, stampv);
  }
  BAR();
  TS(38);
  {
    f32x4 lo = {bflo(hres[0]), bfhi(hres[0]), bflo(hres[1]), bfhi(hres[1])}, hi = {bflo(hres[2]), bfhi(hres[2]), bflo(hres[3]), bfhi(hres[3])};
    const int l2 = __builtin_amdgcn_mbcnt_hi(~0u, __builtin_amdgcn_mbcnt_lo(~0u, 0u)), r2 = l2 & 15, g2 = l2 >> 4;
    const u32 frow2 = (256 * xcd + 2 * (64 * stw + 32 * q2 + 16 * rb + r2) + p2) * 896 + c2 * 128 + ch2 * 64 + g2 * 8;
    LAS char* pa = L + (stw * 8 * 32 + 16 * rb + r2) * PROW + (32 * ch2 + 4 * g2) * 4;
#pragma unroll
    for (int k = 0; k < 8; ++k) {
      lo += *(LAS f32x4*)(pa + k * 32 * PROW);
      hi += *(LAS f32x4*)(pa + k * 32 * PROW + 64);
    }
    *(GAS v2u*)((GAS char*)outg + frow2) = v2u{pk(lo[0], lo[1]), pk(lo[2], lo[3])};
    *(GAS v2u*)((GAS char*)outg + frow2 + 32) = v2u{pk(hi[0], hi[1]), pk(hi[2], hi[3])};
    asm volatile(".Lfb_last:");
    TS(39);
    TC(53);
  }
  TSOUT();
}

extern "C" void kernel_launch(void* const* d_in, const int* in_sizes, int n_in, void* d_out, int out_size, void* d_ws,
                              size_t ws_size, hipStream_t stream) {
  fused_block<<<256, 512, 0, stream>>>(d_in[0], d_in[1], d_in[2], d_in[4], d_in[5], d_in[6], d_out, (char*)d_ws);
}
```

```cpp
#define STAMP 1
#define PW 0
#define PROBE 0
#include <hip/hip_runtime.h>
#include <hip/hip_bf16.h>

typedef unsigned u32;
typedef unsigned long long u64;
typedef u32 v4u __attribute__((ext_vector_type(4)));
typedef u32 v2u __attribute__((ext_vector_type(2)));
typedef short s16x4 __attribute__((ext_vector_type(4)));
typedef short s16x8 __attribute__((ext_vector_type(8)));
typedef __bf16 bf16x2 __attribute__((ext_vector_type(2)));
typedef __bf16 bf16x8 __attribute__((ext_vector_type(8)));
typedef float f32x2 __attribute__((ext_vector_type(2)));
typedef float f32x4 __attribute__((ext_vector_type(4)));
typedef float f32x16 __attribute__((ext_vector_type(16)));
#define GAS __attribute__((address_space(1)))
#define LAS __attribute__((address_space(3)))
#if defined(__HIP_DEVICE_COMPILE__)
#define NOPK __attribute__((target("no-packed-fp32-ops")))
#else
#define NOPK
#endif
#define DI __device__ __forceinline__ NOPK
typedef __amdgpu_buffer_rsrc_t rsrc_t;
template <int V>
struct IC {
  static constexpr int v = V;
};
template <int A, int B, class F>
DI void sfor(F&& f) {
  if constexpr (B - A == 1) {
    f(IC<A>{});
  } else if constexpr (B - A > 1) {
    sfor<A, (A + B) / 2>(f);
    sfor<(A + B) / 2, B>(f);
  }
}
#define LAMBDA __attribute__((always_inline)) NOPK

#define XB_DELAY 0

constexpr u32 WS_FLAG = 4096;
constexpr u32 WS_O = 1024 * 1024;
constexpr u32 WS_H = WS_O + 2 * 1024 * 1024;
constexpr u32 WS_ST = WS_H + 64 * 28 * 1024;
constexpr u32 WS_HH = WS_ST + 256 * 1024;
constexpr u32 WS_END = WS_HH + 64 * 112 * 1024;
constexpr u32 WS_DBG = 16 * 1024 * 1024;
constexpr int FL1 = 0, FL2 = 2048, FL3 = 4096;

constexpr u32 W1M = 448 * 64;
constexpr u32 PL = 14368;
constexpr u32 L_XB = 6 * PL;
constexpr u32 XROW = 928, XBLK = 16 * XROW;
constexpr u32 L_XH = L_XB + 4 * XBLK;
constexpr u32 L_TCH = L_XH + 6144;
constexpr u32 L_EX = L_XH + 8 * XROW;
constexpr u32 L_CS1 = L_EX + 5 * 2048;
constexpr u32 L_PRG = L_CS1 + 384;
constexpr u32 W2PL = 3616, W2Q = 4 * W2PL;
constexpr u32 L_S4 = 8 * PL;
constexpr u32 L_CS3 = L_S4 + 40960;
constexpr u32 PROW = 272;
constexpr u32 LDS_BYTES = 163840;
constexpr int NA4 = 20, NB4 = 8;

#define WAITV_(n) asm volatile("s_waitcnt vmcnt(" #n ")" ::: "memory")
#define WAITV(n) WAITV_(n)
#define BAR() asm volatile("s_waitcnt lgkmcnt(0)\n\ts_barrier" ::: "memory")
#define SB() __builtin_amdgcn_sched_barrier(0)

DI void dma16(u32 lds, u32 voff, rsrc_t rs, u32 soff) {
  asm volatile("s_mov_b32 m0, %0\n\ts_nop 0\n\tbuffer_load_dwordx4 %1, %2, %3 offen lds" ::"s"(lds), "v"(voff), "s"(rs), "s"(soff)
               : "memory");
}
template <int N, int DL, int DS>
DI void dma_run(u32 lds, u32 voff, rsrc_t rs, u32 soff) {
  u32 t;
  asm volatile(
      "s_mov_b32 m0, %1\n\ts_mov_b32 %0, %4\n\t"
      ".rept %c5\n\tbuffer_load_dwordx4 %2, %3, %0 offen lds\n\ts_add_u32 m0, m0, %c6\n\ts_add_u32 %0, %0, %c7\n\t.endr"
      : "=&s"(t)
      : "s"(lds), "v"(voff), "s"(rs), "s"(soff), "i"(N), "i"(DL), "i"(DS)
      : "memory", "scc");
}
template <int N>
DI void dma_run_s(u32 lds, u32 voff, rsrc_t rs, u32 soff, u32 dl, u32 ds) {
  u32 t;
  asm volatile(
      "s_mov_b32 m0, %1\n\ts_mov_b32 %0, %4\n\t"
      ".rept %c5\n\tbuffer_load_dwordx4 %2, %3, %0 offen lds\n\ts_add_u32 m0, m0, %6\n\ts_add_u32 %0, %0, %7\n\t.endr"
      : "=&s"(t)
      : "s"(lds), "v"(voff), "s"(rs), "s"(soff), "i"(N), "s"(dl), "s"(ds)
      : "memory", "scc");
}
template <int N>
DI void waitv() { asm volatile("s_waitcnt vmcnt(%c0)" ::"i"(N) : "memory"); }
DI void st128(v4u v, rsrc_t rs, u32 voff, u32 soff) {
  __builtin_amdgcn_raw_buffer_store_b128(v, rs, voff, soff, 16);
  asm volatile("s_nop 1" ::"v"(v) : "memory");
}
DI rsrc_t mkrs(const void* p, u32 bytes) { return __builtin_amdgcn_make_buffer_rsrc((void*)p, (short)0, (int)bytes, 0x00020000); }
DI v4u trfrag(LAS char* a, LAS char* b) {
  s16x4 lo = __builtin_amdgcn_ds_read_tr16_b64_v4i16((LAS s16x4*)a);
  s16x4 hi = __builtin_amdgcn_ds_read_tr16_b64_v4i16((LAS s16x4*)b);
  return __builtin_bit_cast(v4u, __builtin_shufflevector(lo, hi, 0, 1, 2, 3, 4, 5, 6, 7));
}
DI f32x4 mfma16(v4u a, v4u b, f32x4 c) {
  return __builtin_amdgcn_mfma_f32_16x16x32_bf16(__builtin_bit_cast(bf16x8, a), __builtin_bit_cast(bf16x8, b), c, 0, 0, 0);
}
DI f32x16 mfma32(v4u a, v4u b, f32x16 c) {
  return __builtin_amdgcn_mfma_f32_32x32x16_bf16(__builtin_bit_cast(bf16x8, a), __builtin_bit_cast(bf16x8, b), c, 0, 0, 0);
}
DI float u2f(u32 u) { return __builtin_bit_cast(float, u); }
DI u32 f2u(float f) { return __builtin_bit_cast(u32, f); }
DI u32 pk(float a, float b) {
  f32x2 f = {a, b};
  return __builtin_bit_cast(u32, __builtin_convertvector(f, bf16x2));
}
DI float bflo(u32 u) { return __builtin_bit_cast(float, u << 16); }
DI float bfhi(u32 u) { return __builtin_bit_cast(float, u & 0xffff0000u); }
DI float dot2(u32 a, u32 b, float c) {
  return __builtin_amdgcn_fdot2_f32_bf16(__builtin_bit_cast(bf16x2, a), __builtin_bit_cast(bf16x2, b), c, false);
}
DI float sum16(float x) {
  auto r = __builtin_amdgcn_permlane16_swap(__builtin_bit_cast(u32, x), __builtin_bit_cast(u32, x), false, false);
  const u32 a = r[0], c = r[1];
  return u2f(a) + u2f(c);
}
DI float sum32(float x) {
  auto r = __builtin_amdgcn_permlane32_swap(__builtin_bit_cast(u32, x), __builtin_bit_cast(u32, x), false, false);
  const u32 a = r[0], c = r[1];
  return u2f(a) + u2f(c);
}
DI float max16(float x) {
  auto r = __builtin_amdgcn_permlane16_swap(__builtin_bit_cast(u32, x), __builtin_bit_cast(u32, x), false, false);
  const u32 a = r[0], c = r[1];
  return __builtin_fmaxf(u2f(a), u2f(c));
}
DI float max32(float x) {
  auto r = __builtin_amdgcn_permlane32_swap(__builtin_bit_cast(u32, x), __builtin_bit_cast(u32, x), false, false);
  const u32 a = r[0], c = r[1];
  return __builtin_fmaxf(u2f(a), u2f(c));
}
DI float bperm(int srclane, float x) {
  return __builtin_bit_cast(float, __builtin_amdgcn_ds_bpermute(srclane * 4, __builtin_bit_cast(int, x)));
}
#define RLX_AGENT __ATOMIC_RELAXED, __HIP_MEMORY_SCOPE_AGENT
#define RLX_WG __ATOMIC_RELAXED, __HIP_MEMORY_SCOPE_WORKGROUP
#define WAITL() asm volatile("s_waitcnt lgkmcnt(0)" ::: "memory")
DI void poll(char* ws, int idx, bool act, u64 tag) {
  GAS u64* p = (GAS u64*)(ws + WS_FLAG + (u32)idx * 128u);
#pragma unroll 1
  for (int spins = 0; spins < (1 << 16); ++spins) {
    u64 v = tag;
    if (act) v = __hip_atomic_load(p, RLX_AGENT);
    if (__builtin_amdgcn_ballot_w64(v != tag) == 0) break;
    __builtin_amdgcn_s_sleep(2);
  }
  asm volatile("" ::: "memory");
}
DI void setflag(char* ws, int idx, u64 tag, int lane) {
  if (lane == 0) __hip_atomic_store((GAS u64*)(ws + WS_FLAG + (u32)idx * 128u), tag, RLX_AGENT);
}
DI void prog(LAS char* L, int w, u32 stage) {
  asm volatile("" ::: "memory");
  *(volatile LAS u32*)(L + L_PRG + w * 4) = stage;
  SB();
}
DI void await(LAS char* L, u32 need, int lane) {
  const u32 nd = (need >> (4 * (lane & 7))) & 15u;
#pragma unroll 1
  for (int spins = 0; spins < (1 << 20); ++spins) {
    const u32 v = *(volatile LAS u32*)(L + L_PRG + (lane & 7) * 4);
    if (__builtin_amdgcn_ballot_w64(v < nd) == 0) break;
    __builtin_amdgcn_s_sleep(1);
  }
  asm volatile("" ::: "memory");
}
DI void touch(u32 lds, rsrc_t rs, u32 voff, u32 soff) {
  asm volatile("s_mov_b32 m0, %0\n\ts_nop 0\n\tbuffer_load_dword %1, %2, %3 offen sc1 lds" ::"s"(lds), "v"(voff), "s"(rs), "s"(soff)
               : "memory");
}
#define TS(i)                                                   \
  do {                                                          \
    SB();                                                       \
    const u32 t_ = (u32)__builtin_amdgcn_s_memrealtime();       \
    stampv = lane == (i) ? t_ : stampv;                         \
    asm volatile("" : "+v"(stampv));                            \
    SB();                                                       \
  } while (0)
#define TC(i)                                                   \
  do {                                                          \
    SB();                                                       \
    const u32 t_ = (u32)__builtin_amdgcn_s_memtime();           \
    stampv = lane == (i) ? t_ : stampv;                         \
    asm volatile("" : "+v"(stampv));                            \
    SB();                                                       \
  } while (0)
#define TSOUT()                                                                                         \
  do {                                                                                                  \
    if (w == PW && jj < 28) {                                                                           \
      const u32 t0_ = __builtin_amdgcn_readlane(stampv, 0), tk_ = __builtin_amdgcn_readlane(stampv, PROBE); \
      const u32 until_ = 4000u + 4u * (tk_ - t0_);                                                      \
      for (int i_ = 0; i_ < 100000; ++i_) {                                                             \
        if ((u32)__builtin_amdgcn_s_memrealtime() - t0_ >= until_) break;                               \
        __builtin_amdgcn_s_sleep(2);                                                                    \
      }                                                                                                 \
    }                                                                                                   \
  } while (0)

template <int N>
DI void stage_w4(v4u (&w4f)[NA4][2], LAS char* L, u32 L0, rsrc_t rsWd, u32 so, int w, int lane) {
  const u32 vo = (lane >> 3) * 896 + (((lane & 7) ^ ((lane >> 2) & 4)) * 16);
  const u32 lb = L_S4 + (w & 3) * 10240;
  const int tq = (lane >> 2) & 3;
  LAS char* a0 = L + lb + (4 * (lane >> 5) + tq) * 128 + ((tq >> 1) * 64) + ((lane >> 4) & 1) * 32 + (lane & 3) * 8;
  LAS char* a1 = L + lb + (4 * (lane >> 5) + tq) * 128 + (((tq >> 1) ^ 1) * 64) + ((lane >> 4) & 1) * 32 + (lane & 3) * 8;
  sfor<0, N>([&](auto K_) LAMBDA {
    constexpr int k = decltype(K_)::v;
    constexpr int last = (k + 3 > 4 ? k + 3 : 4) < N - 1 ? (k + 3 > 4 ? k + 3 : 4) : N - 1;
    waitv<2 * (last - k)>();
    w4f[k][0] = trfrag(a0 + (k % 5) * 2048, a0 + (k % 5) * 2048 + 1024);
    w4f[k][1] = trfrag(a1 + (k % 5) * 2048, a1 + (k % 5) * 2048 + 1024);
    if constexpr (k >= 1 && k + 4 < N) {
      asm volatile("s_waitcnt lgkmcnt(4)" ::: "memory");
      dma_run<2, 1024, 8 * 896>(L0 + lb + ((k - 1) % 5) * 2048, vo, rsWd, so + (k + 4) * 16 * 896);
    }
  });
}
template <int N>
DI void phase4(v4u (&w4f)[NA4][2], LAS char* L, char* ws, rsrc_t rsWS, int k0, int t32a, u64 tag, int w, int lane, u32& stampv) {
  const int hh = lane >> 5;
#pragma unroll 1
  for (int st = 0; st < 2; ++st) {
    const int t32 = t32a + 4 * st;
    poll(ws, FL3 + t32 * 28 + (k0 >> 2) + lane, lane < N / 4, tag);
    TS(29 + 4 * st);
    constexpr int D = N < 10 ? N : 10;
    v4u hf[D];
#pragma unroll
    for (int kk = 0; kk < D; ++kk) hf[kk] = __builtin_amdgcn_raw_buffer_load_b128(rsWS, lane * 16, WS_HH + (t32 * 112 + k0 + kk) * 1024, 16);
    SB();
    f32x16 acc[2];
#pragma unroll
    for (int ct = 0; ct < 2; ++ct)
#pragma unroll
      for (int i = 0; i < 16; ++i) acc[ct][i] = 0.f;
#pragma unroll
    for (int kk = 0; kk < N; ++kk) {
      const v4u cur = hf[kk % D];
      acc[0] = mfma32(w4f[kk][0], cur, acc[0]);
      acc[1] = mfma32(w4f[kk][1], cur, acc[1]);
      if (kk + D < N) hf[kk % D] = __builtin_amdgcn_raw_buffer_load_b128(rsWS, lane * 16, WS_HH + (t32 * 112 + k0 + kk + D) * 1024, 16);
      SB();
    }
    TS(30 + 4 * st);
    if (st == 0) BAR();
#pragma unroll
    for (int ct = 0; ct < 2; ++ct)
#pragma unroll
      for (int i = 0; i < 4; ++i)
        *(LAS f32x4*)(L + ((st * 8 + w) * 32 + (lane & 31)) * PROW + (32 * ct + 8 * i + 4 * hh) * 4) =
            f32x4{acc[ct][4 * i], acc[ct][4 * i + 1], acc[ct][4 * i + 2], acc[ct][4 * i + 3]};
    TS(31 + 4 * st);
  }
}
DI v2u load_epoch(rsrc_t rs) {
  v2u e;
  asm volatile("buffer_load_dwordx2 %0, off, %1, 0 sc1\n\ts_waitcnt vmcnt(0)" : "=&v"(e) : "s"(rs) : "memory");
  return e;
}
DI float bcast16(float x) {
  return u2f(__builtin_amdgcn_update_dpp(0, (int)f2u(x), 0x150, 0xf, 0xf, true));
}

NOPK __global__ __launch_bounds__(512, 2) void fused_block(const void* __restrict__ xg, const void* __restrict__ wqkv,
                                                      const void* __restrict__ wo, const void* __restrict__ wg,
                                                      const void* __restrict__ wu, const void* __restrict__ wd,
                                                      void* __restrict__ outg, char* ws) {
  __shared__ __attribute__((aligned(16))) char smem[LDS_BYTES];
  LAS char* L = (LAS char*)smem;
  const u32 L0 = (u32)(size_t)L;
  const u32 tid = __builtin_amdgcn_workitem_id_x();
  const int b = __builtin_amdgcn_workgroup_id_x(), xcd = b & 7, jj = b >> 3;
  const int lane = tid & 63, w = __builtin_amdgcn_readfirstlane(tid >> 6);
  const int r16 = lane & 15, g = lane >> 4, tq = (lane >> 2) & 3, tp = lane & 3, hh = lane >> 5;
  u32 stampv = 0;
  TS(0);
  TC(48);
  u64 pc;
  u32 clen;
  asm volatile("s_getpc_b64 %0\n.Lfb_here:\n\ts_mov_b32 %1, .Lfb_last-.Lfb_here" : "=s"(pc), "=s"(clen));
  if (tid < 240) *(LAS u32*)(L + (tid < 224 ? L_XH : L_PRG - 896) + tid * 4) = tid < 224 ? 0x3f803f80u : 0u;

  const int tile1 = (xcd >> 1) * 4 + (jj & 3), head = (xcd & 1) * 8 + (jj >> 2);
  const int srow0 = 256 * (tile1 >> 1) + (tile1 & 1);
  const u32 vo1 = (lane >> 2) * 2688 + ((lane & 3) ^ (2 * hh)) * 16;
  const u32 so1 = (w - 5) * 896 + ((56 * head) & ~15), l1 = L0 + (w - 5) * W1M;
  const u32 f_l = w < 4 ? L0 + L_XB + w * XBLK : l1, f_v = w < 4 ? lane * 16 : vo1, f_s = w < 4 ? (u32)((srow0 + 32 * w) * 896) : so1;
  const u32 f_dl = w < 4 ? XROW : 1024, f_ds = w < 4 ? 1792 : 16 * 2688;
  asm volatile("" ::"s"(f_l), "v"(f_v), "s"(f_s), "s"(f_dl), "s"(f_ds));
  asm volatile("" ::"s"(xg), "s"(wqkv), "s"(wo), "s"(wg), "s"(wu), "s"(wd), "s"(outg), "s"(ws));
  BAR();
  const rsrc_t rsWS = mkrs(ws, WS_END), rsX = mkrs(xg, 2048 * 896), rsW1 = mkrs(wqkv, 448 * 2688);
  if (w < 4) __builtin_amdgcn_s_setprio(2);
  if (w != 4) {
    if (w > 4 || lane < 56) dma_run_s<16>(f_l, f_v, w < 4 ? rsX : rsW1, f_s, f_dl, f_ds);
    if (w < 4 && lane < 56) {
      const int hr = w == 0 ? 1 : 2 * w;
      dma_run<2, XROW, 1792>(L0 + L_XH + hr * XROW, lane * 16, rsX, (tile1 < 2 ? 0 : srow0 - 16 + 2 * hr) * 896);
    }
  } else {
    if (lane < 8) touch(L0 + L_PRG + 64, mkrs((const void*)pc, clen), jj * 1024 + lane * 128, 0);
  }
  const rsrc_t rsWo = mkrs(wo, 448 * 896), rsWg = mkrs(wg, 448 * 3584), rsWu = mkrs(wu, 448 * 3584), rsWd = mkrs(wd, 1792 * 896);
  const bool tail = jj < 28;
  const int tt2 = jj & 3, c2 = jj >> 2;
  const int T3 = (xcd >> 2) * 4 + (jj & 3), fs3 = (xcd & 3) * 7 + (jj >> 2);
  const int stw = w >> 2;
  const u32 vo3 = (lane >> 3) * 4 * 3584 + (lane & 7) * 16;
  {
    v4u xf[14];
    float nmean = 0.f, rstd = 0.f;
    auto rdx = [&](LAS char* xa) LAMBDA {
#pragma unroll
      for (int ks = 0; ks < 14; ++ks) xf[ks] = *(LAS v4u*)(xa + ks * 64);
    };
    auto stats = [&]() LAMBDA {
      float s1[4] = {0.f, 0.f, 0.f, 0.f}, s2[4] = {0.f, 0.f, 0.f, 0.f};
#pragma unroll
      for (int ks = 0; ks < 14; ++ks)
#pragma unroll
        for (int e = 0; e < 4; ++e) {
          s1[e] = dot2(xf[ks][e], 0x3f803f80u, s1[e]);
          s2[e] = dot2(xf[ks][e], xf[ks][e], s2[e]);
        }
      const float t1 = sum32(sum16((s1[0] + s1[1]) + (s1[2] + s1[3]))), t2 = sum32(sum16((s2[0] + s2[1]) + (s2[2] + s2[3])));
      nmean = t1 * (-1.f / 448.f);
      rstd = __builtin_amdgcn_rsqf(__builtin_fmaxf(t2 * (1.f / 448.f) - nmean * nmean, 0.f) + 1e-5f);
      asm volatile("" : "+v"(nmean), "+v"(rstd));
    };
    LAS char* wa[4];
#pragma unroll
    for (int i = 0; i < 4; ++i) {
      wa[i] = L + (i >> 1) * 2 * W1M + (8 * g + tq) * 64 + (((4 * (i & 1) + tp + (head & 1)) & 7) ^ (4 * (g & 1))) * 8;
      asm volatile("" : "+v"(wa[i]));
    }
    LAS char* xh = L + L_XH + (r16 & 7) * XROW + g * 16;
    if (w <= 4) {
      WAITV(0);
      if (w < 4) prog(L, w, 1);
      else await(L, 0x00001111u, lane);
      TS(2);
      rdx(w < 4 ? L + L_XB + w * XBLK + r16 * XROW + g * 16 : xh);
      if (w < 4) prog(L, w, 2);
      stats();
    }
    if (w == 4) {
      *(LAS f32x2*)(L + L_PRG + 64 + r16 * 8) = f32x2{nmean, rstd};
      prog(L, w, 1);
      await(L, 0x33302222u, lane);
      if (XB_DELAY && !((0x2A54 >> head) & 1)) __builtin_amdgcn_s_sleep(XB_DELAY);
      if (lane < 56) {
#pragma unroll 1
        for (int k = 0; k < 4; ++k) dma_run<16, XROW, 1792>(L0 + L_XB + k * XBLK, lane * 16, rsX, (srow0 + 128 + 32 * k) * 896);
      }
      const u32 tv = lane < 49 ? b * 6272 + lane * 128 : 0x7fff0000u;
      touch(L0 + L_TCH, rsWo, lane < 13 ? b * 1664 + lane * 128 : 0x7fff0000u, 0);
      touch(L0 + L_TCH, rsWg, tv, 0);
      touch(L0 + L_TCH, rsWu, tv, 0);
      touch(L0 + L_TCH, rsWd, tv, 0);
      WAITV(4);
      prog(L, w, 6);
    } else if (w > 4) {
      WAITV(8);
      prog(L, w, 1);
      dma_run<12, 1024, 16 * 2688>(l1 + 16 * 1024, vo1, rsW1, so1 + 256 * 2688);
      __builtin_amdgcn_s_setprio(3);
      await(L, 0x00001111u, lane);
      rdx(xh);
      f32x4 h0 = {0.f, 0.f, 0.f, 0.f}, h1 = {0.f, 0.f, 0.f, 0.f};
      LAS char *a0 = wa[0] + (w - 5) * W1M, *a1 = wa[1] + (w - 5) * W1M;
      auto part = [&](auto A_, auto B_) LAMBDA {
        constexpr int A = decltype(A_)::v, B = decltype(B_)::v;
        v4u f[B - A][2];
#pragma unroll
        for (int ks = A; ks < B; ++ks) {
          f[ks - A][0] = trfrag(a0 + ks * 2048, a0 + ks * 2048 + 256);
          f[ks - A][1] = trfrag(a1 + ks * 2048, a1 + ks * 2048 + 256);
        }
        SB();
#pragma unroll
        for (int ks = A; ks < B; ++ks) {
          h0 = mfma16(f[ks - A][0], xf[ks], h0);
          h1 = mfma16(f[ks - A][1], xf[ks], h1);
        }
        SB();
      };
      WAITV(12);
      prog(L, w, 2);
      part(IC<0>{}, IC<8>{});
      WAITV(4);
      prog(L, w, 3);
      part(IC<8>{}, IC<12>{});
      await(L, 0x00010000u, lane);
      const f32x2 hs = *(LAS f32x2*)(L + L_PRG + 64 + r16 * 8);
      WAITV(0);
      prog(L, w, 4);
      part(IC<12>{}, IC<14>{});
      if (r16 == 0) {
        *(LAS f32x4*)(L + L_CS1 + ((w - 5) * 32 + 4 * g) * 4) = h0;
        *(LAS f32x4*)(L + L_CS1 + ((w - 5) * 32 + 16 + 4 * g) * 4) = h1;
      }
      prog(L, w, 5);
      float t0[4], t1[4];
#pragma unroll
      for (int j = 0; j < 4; ++j) {
        t0[j] = (h0[j] + bcast16(h0[j]) * hs[0]) * hs[1];
        t1[j] = (h1[j] + bcast16(h1[j]) * hs[0]) * hs[1];
      }
      const v2u lo = {pk(t0[0], t0[1]), pk(t0[2], t0[3])}, hi = {g == 3 ? 0u : pk(t1[0], t1[1]), g == 3 ? 0u : pk(t1[2], t1[3])};
      if (w != 5) {
        *(LAS v2u*)(L + L_EX + (w == 6 ? lane * 16 : 1024 + r16 * 64 + g * 8)) = lo;
        *(LAS v2u*)(L + L_EX + (w == 6 ? lane * 16 + 8 : 1024 + r16 * 64 + g * 8 + 32)) = hi;
      }
      prog(L, w, 6);
      __builtin_amdgcn_s_setprio(0);
    }
    if (w >= 4) {
      await(L, 0x66660000u, lane);
      TS(2);
      rdx(L + L_XB + (w & 3) * XBLK + r16 * XROW + g * 16);
      prog(L, w, 7);
    } else {
      await(L, 0x11100000u, lane);
    }
    TS(7);
    f32x4 acc[6];
#pragma unroll
    for (int i = 0; i < 6; ++i) acc[i] = f32x4{0.f, 0.f, 0.f, 0.f};
    constexpr int PD = 5;
    v4u wf[PD + 1];
    auto rdw = [&](int n) LAMBDA {
      const int i = n % 6;
      LAS char* a = (i < 4 ? wa[i & 1] + (i >> 1) * W1M : wa[2 + (i & 1)]) + (n / 6) * 2048;
      return trfrag(a, a + 256);
    };
#pragma unroll
    for (int n = 0; n < PD; ++n) wf[n] = rdw(n);
    SB();
#pragma unroll
    for (int n = 0; n < 84; ++n) {
      if ((n + PD) % 24 == 0 && w < 4) await(L, 0x11100000u * ((n + PD) / 24 + 1), lane);
      if (n + PD < 84) wf[(n + PD) % (PD + 1)] = rdw(n + PD);
      acc[n % 6] = mfma16(wf[n % (PD + 1)], xf[n / 6], acc[n % 6]);
      SB();
    }
    TS(3);
    if (w >= 4) {
      prog(L, w, 8);
      stats();
      if (tail) {
        await(L, 0x88883333u, lane);
        dma_run<7, 1024, 32 * 3584>(L0 + (w - 4) * PL, vo3, rsWg, (w - 4) * 3584 + fs3 * 128);
      }
    }
    f32x4 cs[6];
#pragma unroll 1
    for (int spins = 0; spins < (1 << 20); ++spins) {
      const u32 f = *(volatile LAS u32*)(L + L_PRG + (lane & 7) * 4);
#pragma unroll
      for (int i = 0; i < 6; ++i) cs[i] = *(volatile LAS f32x4*)(L + L_CS1 + (16 * i + 4 * g) * 4);
      if (__builtin_amdgcn_ballot_w64((lane & 7) >= 5 && f < 5) == 0) break;
      __builtin_amdgcn_s_sleep(1);
    }
    auto nrm = [&](int i, float r) LAMBDA {
      v2u o = {pk((acc[i][0] + nmean * cs[i][0]) * r, (acc[i][1] + nmean * cs[i][1]) * r),
               pk((acc[i][2] + nmean * cs[i][2]) * r, (acc[i][3] + nmean * cs[i][3]) * r)};
      if (i & 1) o = g == 3 ? v2u{0u, 0u} : o;
      return o;
    };
    const u32 kw = w < 4 ? L_EX + (w + 1) * 2048 : L_XH + (w - 4) * 2048, vw = w == 7 ? L_XH + 6400 : kw + 1024;
    const u32 kr = w <= 4 ? L_EX + w * 2048 : L_XH + (w - 5) * 2048;
    const v2u k0 = nrm(2, rstd), k1 = nrm(3, rstd);
    const v4u kf = {k0[0], k0[1], k1[0], k1[1]};
    if (w != 7) *(LAS v4u*)(L + kw + lane * 16) = kf;
    *(LAS v2u*)(L + vw + r16 * 64 + g * 8) = nrm(4, rstd);
    *(LAS v2u*)(L + vw + r16 * 64 + g * 8 + 32) = nrm(5, rstd);
    prog(L, w, w < 4 ? 3 : 9);
    const float rq = rstd * (0.18898223650461362f * 1.4426950408889634f);
    const v2u q0 = nrm(0, rq), q1 = nrm(1, rq);
    const v4u qf = {q0[0], q0[1], q1[0], q1[1]};
    const f32x4 z = {0.f, 0.f, 0.f, 0.f};
    const f32x4 so = mfma16(kf, qf, z);
    if (w >= 4 && tail) dma_run<7, 1024, 32 * 3584>(L0 + (w - 4) * PL + 7168, vo3, rsWg, (w - 4 + 224) * 3584 + fs3 * 128);
    const u32 pneed = w == 0 ? 0x66000000u : (w <= 4 ? 3u : 9u) << (4 * (w - 1));
    const u32 nd = (pneed >> (4 * (lane & 7))) & 15u;
    const u32 vt = (4 * g + tq) * 64 + tp * 8;
    v4u kp, v0, v1;
#pragma unroll 1
    for (int spins = 0; spins < (1 << 20); ++spins) {
      const u32 f = *(volatile LAS u32*)(L + L_PRG + (lane & 7) * 4);
      asm volatile("" ::: "memory");
      kp = *(LAS v4u*)(L + kr + lane * 16);
      v0 = trfrag(L + kr + 1024 + vt, L + vw + vt);
      v1 = trfrag(L + kr + 1024 + vt + 32, L + vw + vt + 32);
      asm volatile("" ::: "memory");
      if (__builtin_amdgcn_ballot_w64(f < nd) == 0) break;
      __builtin_amdgcn_s_sleep(1);
    }
    TS(4);
    const f32x4 sp = mfma16(kp, qf, z);
    const bool noprev = (tile1 < 2) && (w == 0);
    float p[8];
    float mx = -3.0e38f;
#pragma unroll
    for (int j = 0; j < 4; ++j) {
      const int d = r16 - (4 * g + j);
      p[j] = (d <= -9 && !noprev) ? sp[j] : -3.0e38f;
      p[4 + j] = (d >= 0 && d <= 7) ? so[j] : -3.0e38f;
      mx = __builtin_fmaxf(mx, __builtin_fmaxf(p[j], p[4 + j]));
    }
    mx = max32(max16(mx));
    float sum = 0.f;
#pragma unroll
    for (int j = 0; j < 8; ++j) {
      p[j] = __builtin_amdgcn_exp2f(p[j] - mx);
      sum += p[j];
    }
    const v4u pf = {pk(p[0], p[1]), pk(p[2], p[3]), pk(p[4], p[5]), pk(p[6], p[7])};
    const f32x4 o0 = mfma16(v0, pf, z), o1 = mfma16(v1, pf, z);
    sum = sum32(sum16(sum));
    const float inv = __builtin_amdgcn_rcpf(sum);
    const v4u of = {pk(o0[0] * inv, o0[1] * inv), pk(o0[2] * inv, o0[3] * inv), pk(o1[0] * inv, o1[1] * inv), pk(o1[2] * inv, o1[3] * inv)};
    st128(of, rsWS, lane * 16, WS_O + ((tile1 * 8 + w) * 16 + head) * 1024);
    TS(5);
  }
  u64 tag;
  {
    const v2u epoch = load_epoch(rsWS);
    const u32 lo = __builtin_amdgcn_readfirstlane(epoch[0]), hi = __builtin_amdgcn_readfirstlane(epoch[1]);
    tag = (((u64)hi << 32) | lo) * 6364136223846793005ull + 1442695040888963407ull;
  }
  setflag(ws, FL1 + (tile1 * 8 + w) * 16 + head, tag, lane);
  TS(6);
  if (!tail) {
    if (w < 4) {
#pragma unroll 1
      for (int k = 0; k < 7; ++k) {
        const u32 r = ((k * 4 + (jj - 28)) * 2 + (w >> 1)) * 8 + (lane >> 3);
        touch(L0 + L_TCH, (w & 1) ? rsWu : rsWg, (lane & 7) < 7 ? r * 3584 + ((xcd & 3) * 7 + (lane & 7)) * 128 : 0x7fff0000u, 0);
      }
    }
#pragma unroll 1
    for (int k = 0; k < 7; ++k) {
      const u32 idx = ((k * 4 + (jj - 28)) * 8 + w) * 64 + lane;
      touch(L0 + L_TCH, rsWd, idx < 12544 ? (idx < 3584 ? idx + 8960 : idx - 3584) * 128 : 0x7fff0000u, 0);
    }
    if (b == 255) {
#pragma unroll 1
      for (int k = 0; k < 4; ++k) poll(ws, FL1 + (w * 4 + k) * 64 + lane, true, tag);
      if (lane == 0) {
        u32 old = __hip_atomic_fetch_add((LAS u32*)(L + L_PRG + 32), 1u, RLX_WG);
        if (old == 7) __hip_atomic_store((GAS u64*)ws, tag, RLX_AGENT);
      }
    }
    TSOUT();
    return;
  }

  const int p2 = tt2 >> 1, q2 = tt2 & 1, rb = w & 1, ch2 = (w >> 1) & 1;
  const u32 frow = (256 * xcd + 2 * (64 * stw + 32 * q2 + 16 * rb + r16) + p2) * 896 + c2 * 128 + ch2 * 64 + g * 8;
  const int k04 = w < 4 ? NA4 * w : 4 * NA4 + NB4 * (w - 4);
  const u32 so4 = 16 * k04 * 896 + c2 * 128;
  v4u hres;
  if (w >= 4) __builtin_amdgcn_s_setprio(3);
  {
    const v2u xa = __builtin_amdgcn_raw_buffer_load_b64(rsX, frow, 0, 0), xb = __builtin_amdgcn_raw_buffer_load_b64(rsX, frow, 32, 0);
    SB();
    const int blk = (xcd * 2 + p2) * 8 + 4 * stw + 2 * q2 + rb;
    v4u of[16], w2f[16][2];
    auto load_o = [&]() __attribute__((always_inline)) NOPK {
#pragma unroll
      for (int hd = 0; hd < 16; ++hd) of[hd] = __builtin_amdgcn_raw_buffer_load_b128(rsWS, lane * 16, WS_O + (blk * 16 + hd) * 1024, 16);
      SB();
    };
    if (w < 4) {
      const u32 vo2 = (lane >> 3) * 4 * 896 + (lane & 7) * 16;
      await(L, 0x77770000u, lane);
      const u32 l2 = L0 + L_XB + w * W2Q, s2 = 112 * w * 896 + c2 * 128;
      dma_run<4, W2PL, 896>(l2, vo2, rsWo, s2);
      dma_run<4, W2PL, 896>(l2 + 1024, vo2, rsWo, s2 + 32 * 896);
      dma_run<4, W2PL, 896>(l2 + 2048, vo2, rsWo, s2 + 64 * 896);
      if (lane < 32) dma_run<4, W2PL, 896>(l2 + 3072, vo2, rsWo, s2 + 96 * 896);
      TS(16);
      WAITV(0);
      prog(L, w, 4);
      poll(ws, FL1 + blk * 16 + lane, lane < 16, tag);
      TS(17);
      load_o();
    } else {
      dma_run<7, 1024, 32 * 3584>(L0 + (4 + ((w >> 1) & 1)) * PL + (w & 1) * 7168, vo3, rsWu, (((w >> 1) & 1) + (w & 1) * 224) * 3584 + fs3 * 128);
    }
    await(L, 0x00004444u, lane);
    {
      LAS char* a = L + L_XB + tq * W2PL + g * 128 + tp * 8 + ch2 * 64;
      LAS char* a2 = a + (g < 3 ? 512 : 0);
#pragma unroll
      for (int hd = 0; hd < 16; ++hd)
#pragma unroll
        for (int cb = 0; cb < 2; ++cb) {
          const u32 o = (hd >> 2) * W2Q + (hd & 3) * 896 + cb * 32;
          w2f[hd][cb] = trfrag(a + o, a2 + o);
        }
    }
    prog(L, w, w < 4 ? 5 : 10);
    if (w >= 4) {
      await(L, 0xAAAA5555u, lane);
      dma_run<7, 1024, 32 * 3584>(L0 + (6 + ((w >> 1) & 1)) * PL + (w & 1) * 7168, vo3, rsWu,
                                  ((2 + ((w >> 1) & 1)) + (w & 1) * 224) * 3584 + fs3 * 128);
      dma_run<10, 1024, 8 * 896>(L0 + L_S4 + (w & 3) * 10240, (lane >> 3) * 896 + (((lane & 7) ^ ((lane >> 2) & 4)) * 16), rsWd, so4);
      WAITV(10);
      prog(L, w, 11);
      await(L, 0xBBBB0000u, lane);
      {
        f32x4 cg = {0.f, 0.f, 0.f, 0.f}, cu = {0.f, 0.f, 0.f, 0.f};
        const v4u ones = {0x3f803f80u, 0x3f803f80u, 0x3f803f80u, 0x3f803f80u};
        LAS char* ag = L + tq * PL + g * 128 + tp * 8 + (w - 4) * 32;
        LAS char* au = ag + 4 * PL;
        asm volatile("" : "+v"(au));
        v4u f[6];
        sfor<0, 34>([&](auto N_) LAMBDA {
          constexpr int n = decltype(N_)::v;
          if constexpr (n >= 6) {
            if constexpr (n & 1) cu = mfma16(ones, f[n % 6], cu);
            else cg = mfma16(ones, f[n % 6], cg);
          }
          if constexpr (n < 28) {
            LAS char* a = ((n & 1) ? au : ag) + (n >> 1) * 1024;
            f[n % 6] = trfrag(a, a + 512);
          }
          SB();
        });
        const float c = (g & 1) ? cu[0] : cg[0];
        const u32 hi = pk(c, c);
        const u32 d0 = pk(bflo(hi), c - bflo(hi));
        *(LAS v4u*)(L + L_CS3 + (w - 4) * 1024 + lane * 16) = v4u{hh ? 0u : d0, hh ? 0u : (hi & 0xffffu), 0u, 0u};
        prog(L, w, 12);
      }
      TS(16);
      poll(ws, FL1 + blk * 16 + lane, lane < 16, tag);
      TS(17);
      load_o();
    }
    f32x4 a0 = {bflo(xa[0]), bfhi(xa[0]), bflo(xa[1]), bfhi(xa[1])}, a1 = {bflo(xb[0]), bfhi(xb[0]), bflo(xb[1]), bfhi(xb[1])};
#pragma unroll
    for (int hd = 0; hd < 16; ++hd) {
      a0 = mfma16(w2f[hd][0], of[hd], a0);
      a1 = mfma16(w2f[hd][1], of[hd], a1);
    }
    TS(18);
    hres = v4u{pk(a0[0], a0[1]), pk(a0[2], a0[3]), pk(a1[0], a1[1]), pk(a1[2], a1[3])};
    const int t32 = (xcd * 2 + stw) * 4 + tt2;
    st128(hres, rsWS, (g >> 1) * 1024 + ((g & 1) * 32 + 16 * rb + r16) * 16,
                                           WS_H + (t32 * 28 + 4 * c2 + 2 * ch2) * 1024);
    float s1 = 0.f, s2 = 0.f;
#pragma unroll
    for (int i = 0; i < 4; ++i) {
      const float lo = bflo(hres[i]), hi = bfhi(hres[i]);
      s1 += lo + hi;
      s2 += lo * lo + hi * hi;
    }
    s1 = sum32(sum16(s1));
    s2 = sum32(sum16(s2));
    if (lane < 16)
      __builtin_amdgcn_raw_buffer_store_b64(v2u{f2u(s1), f2u(s2)}, rsWS, (16 * rb + r16) * 8,
                                            WS_ST + (t32 * 14 + 2 * c2 + ch2) * 256, 16);
    TS(19);
    WAITV(0);
    setflag(ws, FL2 + t32 * 28 + c2 * 4 + (w & 3), tag, lane);
    TS(20);
  }

  v4u w4f[NA4][2];
  LAS char* a3 = L + (4 * (g & 1) + tq) * PL + hh * 128 + tp * 8;
  if (w < 4) {
    await(L, 0xBBBB0000u, lane);
#pragma unroll
    for (int kk = 0; kk < NB4; ++kk) w4f[kk][0] = w4f[kk][1] = v4u{0u, 0u, 0u, 0u};
  } else {
    stage_w4<NB4>(w4f, L, L0, rsWd, so4, w, lane);
    prog(L, w, 13);
  }
  TS(21);
  {
    const int t32 = (T3 * 2 + stw) * 4 + (w & 3);
    poll(ws, FL2 + t32 * 28 + lane, lane < 28, tag);
    TS(24);
    TC(49);
    constexpr int HD = 14, PD = 4;
    v4u hb[HD];
#pragma unroll
    for (int ks = 0; ks < HD; ++ks) hb[ks] = __builtin_amdgcn_raw_buffer_load_b128(rsWS, lane * 16, WS_H + (t32 * 28 + ks) * 1024, 16);
    v2u sv[7];
#pragma unroll
    for (int i = 0; i < 7; ++i) sv[i] = __builtin_amdgcn_raw_buffer_load_b64(rsWS, lane * 8, WS_ST + (t32 * 14 + 2 * i) * 256, 16);
    if (w >= 4) __builtin_amdgcn_s_setprio(1);
    f32x16 acc[4];
#pragma unroll
    for (int c = 0; c < 4; ++c)
#pragma unroll
      for (int i = 0; i < 16; ++i) acc[c][i] = 0.f;
    v4u wf[PD + 1];
    auto rdw = [&](int n) __attribute__((always_inline)) NOPK {
      const int ks = n < 68 ? n >> 2 : 17 + (n - 68) % 12, c = n < 68 ? n & 3 : (n - 68) / 12;
      if (ks == 28) return *(LAS v4u*)(L + L_CS3 + c * 1024 + lane * 16);
      const u32 o = (ks >> 1) * 1024 + (ks & 1) * 256 + c * 32;
      return trfrag(a3 + o, a3 + o + 512);
    };
#pragma unroll
    for (int n = 0; n < PD; ++n) wf[n] = rdw(n);
    float s1 = 0.f, s2 = 0.f, ng = 0.f, irs = 0.f, o[32];
    v4u mf = {0u, 0u, 0u, 0u};
    SB();
    sfor<0, 134>([&](auto N_) LAMBDA {
      constexpr int n = decltype(N_)::v;
      if (n == 4) TC(50);
      if (n == 66) await(L, 0xCCCC0000u, lane);
      if constexpr (n < 116) {
        constexpr int ks = n < 68 ? n >> 2 : 17 + (n - 68) % 12, c = n < 68 ? n & 3 : (n - 68) / 12;
        if (n + PD < 116) wf[(n + PD) % (PD + 1)] = rdw(n + PD);
        acc[c] = mfma32(wf[n % (PD + 1)], ks == 28 ? mf : hb[ks % HD], acc[c]);
        if (n < 68 && c == 3 && ks + HD < 28)
          hb[ks % HD] = __builtin_amdgcn_raw_buffer_load_b128(rsWS, lane * 16, WS_H + (t32 * 28 + ks + HD) * 1024, 16);
      }
      if constexpr (n >= 50 && n < 57) {
        s1 += u2f(sv[n - 50][0]);
        s2 += u2f(sv[n - 50][1]);
      }
      if (n == 58) {
        s1 = sum32(s1) * (-1.f / 448.f);
        s2 = sum32(s2);
      }
      if (n == 60) {
        const float var = __builtin_fmaxf(s2 * (1.f / 448.f) - s1 * s1, 0.f) + 1e-5f;
        const float rstd = __builtin_amdgcn_rsqf(var);
        ng = -rstd * 1.4426950408889634f;
        irs = var * rstd;
      }
      if (n == 62) {
        const u32 hi = pk(s1, s1);
        mf[0] = hh ? 0u : hi;
        mf[1] = hh ? 0u : pk(s1 - bflo(hi), 0.f);
      }
      sfor<0, 32>([&](auto V_) LAMBDA {
        constexpr int V = decltype(V_)::v, c = V >> 3, i = V & 7, d = n - (81 + 3 * V / 2);
        if constexpr (d == 0) o[V] = acc[c][i] * ng;
        if constexpr (d == 1) o[V] = __builtin_amdgcn_exp2f(o[V]);
        if constexpr (d == 2) o[V] = o[V] * irs + irs;
        if constexpr (d == 3) o[V] = __builtin_amdgcn_rcpf(o[V]);
        if constexpr (d == 4) o[V] *= acc[c][8 + i];
        if constexpr (d == 5 && i == 7)
          st128(v4u{pk(o[V - 7], o[V - 6]), pk(o[V - 5], o[V - 4]), pk(o[V - 3], o[V - 2]), pk(o[V - 1], o[V])}, rsWS, lane * 16,
                WS_HH + (t32 * 112 + fs3 * 4 + c) * 1024);
      });
      if (n < 116) SB();
      if (n == 115) {
        TS(25);
        TC(51);
      }
    });
    TS(26);
    TC(52);
    if (w < 4) {
      await(L, 0xDDDD0000u, lane);
      dma_run<10, 1024, 8 * 896>(L0 + L_S4 + w * 10240, (lane >> 3) * 896 + (((lane & 7) ^ ((lane >> 2) & 4)) * 16), rsWd, so4);
      WAITV(10);
    } else {
      WAITV(0);
    }
    setflag(ws, FL3 + t32 * 28 + fs3, tag, lane);
    TS(27);
  }
  if (w < 4) {
    stage_w4<NA4>(w4f, L, L0, rsWd, so4, w, lane);
    __builtin_amdgcn_s_setprio(0);
    TS(28);
    phase4<NA4>(w4f, L, ws, rsWS, k04, xcd * 8 + tt2, tag, w, lane, stampv);
  } else {
    phase4<NB4>(w4f, L, ws, rsWS, k04, xcd * 8 + tt2, tag, w, lane, stampv);
  }
  BAR();
  TS(38);
  {
    f32x4 lo = {bflo(hres[0]), bfhi(hres[0]), bflo(hres[1]), bfhi(hres[1])}, hi = {bflo(hres[2]), bfhi(hres[2]), bflo(hres[3]), bfhi(hres[3])};
    const int l2 = __builtin_amdgcn_mbcnt_hi(~0u, __builtin_amdgcn_mbcnt_lo(~0u, 0u)), r2 = l2 & 15, g2 = l2 >> 4;
    const u32 frow2 = (256 * xcd + 2 * (64 * stw + 32 * q2 + 16 * rb + r2) + p2) * 896 + c2 * 128 + ch2 * 64 + g2 * 8;
    LAS char* pa = L + (stw * 8 * 32 + 16 * rb + r2) * PROW + (32 * ch2 + 4 * g2) * 4;
#pragma unroll
    for (int k = 0; k < 8; ++k) {
      lo += *(LAS f32x4*)(pa + k * 32 * PROW);
      hi += *(LAS f32x4*)(pa + k * 32 * PROW + 64);
    }
    *(GAS v2u*)((GAS char*)outg + frow2) = v2u{pk(lo[0], lo[1]), pk(lo[2], lo[3])};
    *(GAS v2u*)((GAS char*)outg + frow2 + 32) = v2u{pk(hi[0], hi[1]), pk(hi[2], hi[3])};
    asm volatile(".Lfb_last:");
    TS(39);
    TC(53);
  }
  TSOUT();
}

extern "C" void kernel_launch(void* const* d_in, const int* in_sizes, int n_in, void* d_out, int out_size, void* d_ws,
                              size_t ws_size, hipStream_t stream) {
  fused_block<<<256, 512, 0, stream>>>(d_in[0], d_in[1], d_in[2], d_in[4], d_in[5], d_in[6], d_out, (char*)d_ws);
}
```

```cpp
#include <hip/hip_runtime.h>
#include <hip/hip_bf16.h>

typedef unsigned u32;
typedef unsigned long long u64;
typedef u32 v4u __attribute__((ext_vector_type(4)));
typedef u32 v2u __attribute__((ext_vector_type(2)));
typedef u64 v8q __attribute__((ext_vector_type(8)));
typedef short s16x4 __attribute__((ext_vector_type(4)));
typedef short s16x8 __attribute__((ext_vector_type(8)));
typedef __bf16 bf16x2 __attribute__((ext_vector_type(2)));
typedef __bf16 bf16x8 __attribute__((ext_vector_type(8)));
typedef float f32x2 __attribute__((ext_vector_type(2)));
typedef float f32x4 __attribute__((ext_vector_type(4)));
typedef float f32x16 __attribute__((ext_vector_type(16)));
#define GAS __attribute__((address_space(1)))
#define LAS __attribute__((address_space(3)))
#if defined(__HIP_DEVICE_COMPILE__)
#define NOPK __attribute__((target("no-packed-fp32-ops")))
#else
#define NOPK
#endif
#define DI __device__ __forceinline__ NOPK
typedef __amdgpu_buffer_rsrc_t rsrc_t;
template <int V>
struct IC {
  static constexpr int v = V;
};
template <int A, int B, class F>
DI void sfor(F&& f) {
  if constexpr (B - A == 1) {
    f(IC<A>{});
  } else if constexpr (B - A > 1) {
    sfor<A, (A + B) / 2>(f);
    sfor<(A + B) / 2, B>(f);
  }
}
#define LAMBDA __attribute__((always_inline)) NOPK

#define XB_DELAY 12
#define W3EARLY 10
#define PSLEEP 4
#define PD1 4
#define TRE 4
#define CPF 24
#define PD2 4
#define W4T 8
#define W4H 14
#define PD3 12
#define LDELAY 14
#define TOUCH1 1
#define TOUCH 1
#define STAMP 0

constexpr u32 WS_FLAG = 4096;
constexpr u32 WS_O = 1024 * 1024;
constexpr u32 WS_H = WS_O + 2 * 1024 * 1024;
constexpr u32 WS_ST = WS_H + 64 * 28 * 1024;
constexpr u32 WS_HH = WS_ST + 256 * 1024;
constexpr u32 WS_END = WS_HH + 64 * 112 * 1024;
constexpr u32 WS_DBG = 16 * 1024 * 1024;
constexpr int FL1 = 0, FL2 = 256, FL3 = 768;

constexpr u32 XROW = 912;
constexpr u32 L_X = 36864;
constexpr u32 L_XH = 153600;
constexpr u32 PL = 14368;
constexpr u32 L_W2 = 6 * PL;
constexpr u32 W2PL = 3616, W2Q = 4 * W2PL;
constexpr u32 L_Q = 0;
constexpr u32 L_K = L_W2 + 4 * W2Q;
constexpr u32 L_V = L_K + 9 * 1024;
constexpr u32 L_RS = L_V + 9 * 1024;
constexpr u32 L_CS1 = L_RS + 544;
constexpr u32 L_TCH = L_CS1 + 384;
constexpr u32 L_PRG = L_TCH + 256;
constexpr u32 L_S4 = 8 * PL;
constexpr u32 L_T4 = L_S4 + 32768;
constexpr u32 L_CS3 = L_RS;
constexpr u32 PROW = 272;
constexpr u32 LDS_BYTES = 163840;
constexpr int NA4 = 16, NB4 = 12;

#define WAITV_(n) asm volatile("s_waitcnt vmcnt(" #n ")" ::: "memory")
#define WAITV(n) WAITV_(n)
#define BAR() asm volatile("s_waitcnt lgkmcnt(0)\n\ts_barrier" ::: "memory")
#define SB() __builtin_amdgcn_sched_barrier(0)

DI void dma16(u32 lds, u32 voff, rsrc_t rs, u32 soff) {
  asm volatile("s_mov_b32 m0, %0\n\ts_nop 0\n\tbuffer_load_dwordx4 %1, %2, %3 offen lds" ::"s"(lds), "v"(voff), "s"(rs), "s"(soff)
               : "memory");
}
template <int N, int DL, int DS>
DI void dma_run(u32 lds, u32 voff, rsrc_t rs, u32 soff) {
  u32 t;
  asm volatile(
      "s_mov_b32 m0, %1\n\ts_mov_b32 %0, %4\n\t"
      ".rept %c5\n\tbuffer_load_dwordx4 %2, %3, %0 offen lds\n\ts_add_u32 m0, m0, %c6\n\ts_add_u32 %0, %0, %c7\n\t.endr"
      : "=&s"(t)
      : "s"(lds), "v"(voff), "s"(rs), "s"(soff), "i"(N), "i"(DL), "i"(DS)
      : "memory", "scc");
}
template <int N>
DI void dma_run_s(u32 lds, u32 voff, rsrc_t rs, u32 soff, u32 dl, u32 ds) {
  u32 t;
  asm volatile(
      "s_mov_b32 m0, %1\n\ts_mov_b32 %0, %4\n\t"
      ".rept %c5\n\tbuffer_load_dwordx4 %2, %3, %0 offen lds\n\ts_add_u32 m0, m0, %6\n\ts_add_u32 %0, %0, %7\n\t.endr"
      : "=&s"(t)
      : "s"(lds), "v"(voff), "s"(rs), "s"(soff), "i"(N), "s"(dl), "s"(ds)
      : "memory", "scc");
}
DI void dma_loop(u32 lds, u32 voff, rsrc_t rs, u32 soff, u32 dl, u32 ds, u32 nm1) {
  asm volatile(
      "s_mov_b32 m0, %2\n"
      "1:\n\ts_sub_u32 %1, %1, 1\n\tbuffer_load_dwordx4 %3, %4, %0 offen lds\n\ts_cbranch_scc1 2f\n\t"
      "s_add_u32 m0, m0, %5\n\ts_add_u32 %0, %0, %6\n\ts_branch 1b\n2:"
      : "+s"(soff), "+s"(nm1)
      : "s"(lds), "v"(voff), "s"(rs), "s"(dl), "s"(ds)
      : "memory", "scc");
}
DI void dma_x(u32 lds, u32 v0, u32 v1, rsrc_t rs, u32 soff) {
  u32 n, t;
  asm volatile(
      "s_mov_b32 m0, %3\n\ts_mov_b32 %1, 6\n"
      "1:\n\ts_add_u32 %2, %0, 28672\n\tbuffer_load_dwordx4 %4, %6, %0 offen lds\n\ts_add_u32 m0, m0, 1024\n\ts_nop 0\n\t"
      "buffer_load_dwordx4 %5, %6, %0 offen lds\n\ts_add_u32 m0, m0, 1024\n\ts_add_u32 %0, %0, 128\n\t"
      "buffer_load_dwordx4 %4, %6, %2 offen lds\n\ts_add_u32 m0, m0, 1024\n\ts_nop 0\n\t"
      "buffer_load_dwordx4 %5, %6, %2 offen lds\n\ts_add_u32 m0, m0, 1024\n\ts_sub_u32 %1, %1, 1\n\ts_cbranch_scc0 1b"
      : "+s"(soff), "=&s"(n), "=&s"(t)
      : "s"(lds), "v"(v0), "v"(v1), "s"(rs)
      : "memory", "scc");
}
template <int N>
DI void waitv() { asm volatile("s_waitcnt vmcnt(%c0)" ::"i"(N) : "memory"); }
DI void st128(v4u v, rsrc_t rs, u32 voff, u32 soff) {
  __builtin_amdgcn_raw_buffer_store_b128(v, rs, voff, soff, 16);
  asm volatile("s_nop 1" ::"v"(v) : "memory");
}
DI rsrc_t mkrs(const void* p, u32 bytes) { return __builtin_amdgcn_make_buffer_rsrc((void*)p, (short)0, (int)bytes, 0x00020000); }
DI v4u trfrag(LAS char* a, LAS char* b) {
  s16x4 lo = __builtin_amdgcn_ds_read_tr16_b64_v4i16((LAS s16x4*)a);
  s16x4 hi = __builtin_amdgcn_ds_read_tr16_b64_v4i16((LAS s16x4*)b);
  return __builtin_bit_cast(v4u, __builtin_shufflevector(lo, hi, 0, 1, 2, 3, 4, 5, 6, 7));
}
DI f32x4 mfma16(v4u a, v4u b, f32x4 c) {
  return __builtin_amdgcn_mfma_f32_16x16x32_bf16(__builtin_bit_cast(bf16x8, a), __builtin_bit_cast(bf16x8, b), c, 0, 0, 0);
}
DI f32x16 mfma32(v4u a, v4u b, f32x16 c) {
  return __builtin_amdgcn_mfma_f32_32x32x16_bf16(__builtin_bit_cast(bf16x8, a), __builtin_bit_cast(bf16x8, b), c, 0, 0, 0);
}
DI float u2f(u32 u) { return __builtin_bit_cast(float, u); }
DI u32 f2u(float f) { return __builtin_bit_cast(u32, f); }
DI u32 pk(float a, float b) {
  f32x2 f = {a, b};
  return __builtin_bit_cast(u32, __builtin_convertvector(f, bf16x2));
}
DI float bflo(u32 u) { return __builtin_bit_cast(float, u << 16); }
DI float bfhi(u32 u) { return __builtin_bit_cast(float, u & 0xffff0000u); }
DI float dot2(u32 a, u32 b, float c) {
  return __builtin_amdgcn_fdot2_f32_bf16(__builtin_bit_cast(bf16x2, a), __builtin_bit_cast(bf16x2, b), c, false);
}
DI float sum16(float x) {
  auto r = __builtin_amdgcn_permlane16_swap(__builtin_bit_cast(u32, x), __builtin_bit_cast(u32, x), false, false);
  const u32 a = r[0], c = r[1];
  return u2f(a) + u2f(c);
}
DI float sum32(float x) {
  auto r = __builtin_amdgcn_permlane32_swap(__builtin_bit_cast(u32, x), __builtin_bit_cast(u32, x), false, false);
  const u32 a = r[0], c = r[1];
  return u2f(a) + u2f(c);
}
DI float max16(float x) {
  auto r = __builtin_amdgcn_permlane16_swap(__builtin_bit_cast(u32, x), __builtin_bit_cast(u32, x), false, false);
  const u32 a = r[0], c = r[1];
  return __builtin_fmaxf(u2f(a), u2f(c));
}
DI float max32(float x) {
  auto r = __builtin_amdgcn_permlane32_swap(__builtin_bit_cast(u32, x), __builtin_bit_cast(u32, x), false, false);
  const u32 a = r[0], c = r[1];
  return __builtin_fmaxf(u2f(a), u2f(c));
}
DI float bperm(int srclane, float x) {
  return __builtin_bit_cast(float, __builtin_amdgcn_ds_bpermute(srclane * 4, __builtin_bit_cast(int, x)));
}
#define RLX_AGENT __ATOMIC_RELAXED, __HIP_MEMORY_SCOPE_AGENT
#define RLX_WG __ATOMIC_RELAXED, __HIP_MEMORY_SCOPE_WORKGROUP
#define WAITL() asm volatile("s_waitcnt lgkmcnt(0)" ::: "memory")
DI void poll(char* ws, int idx, bool act, u64 tag) {
  GAS u64* p = (GAS u64*)(ws + WS_FLAG + (u32)idx * 128u);
#pragma unroll 1
  for (int spins = 0; spins < (1 << 16); ++spins) {
    if (act) act = __hip_atomic_load(p, RLX_AGENT) != tag;
    if (__builtin_amdgcn_ballot_w64(act) == 0) break;
    __builtin_amdgcn_s_sleep(PSLEEP);
  }
  asm volatile("" ::: "memory");
}
DI void setflag(char* ws, int idx, u64 tag) { __hip_atomic_store((GAS u64*)(ws + WS_FLAG + (u32)idx * 128u), tag, RLX_AGENT); }
DI void arrive_flag(LAS char* L, int cnt, u32 n, char* ws, int idx, u64 tag, int lane) {
  if (lane == 0) {
    const u32 old = __hip_atomic_fetch_add((LAS u32*)(L + L_PRG + 32 + cnt * 4), 1u, RLX_WG);
    if (old == n - 1) __hip_atomic_store((GAS u64*)(ws + WS_FLAG + (u32)idx * 128u), tag, RLX_AGENT);
  }
}
DI void post(LAS char* L, int k) {
  asm volatile("" ::: "memory");
  *(volatile LAS u32*)(L + L_PRG + 128 + k * 4) = 1u;
  SB();
}
DI void pend(LAS char* L, int k) {
#pragma unroll 1
  for (int spins = 0; spins < (1 << 22); ++spins) {
    if (__builtin_amdgcn_readfirstlane(*(volatile LAS u32*)(L + L_PRG + 128 + k * 4)) != 0) break;
    __builtin_amdgcn_s_sleep(1);
  }
  asm volatile("" ::: "memory");
}
DI void prog(LAS char* L, int w, u32 stage) {
  asm volatile("" ::: "memory");
  *(volatile LAS u32*)(L + L_PRG + w * 4) = stage;
  SB();
}
DI void await(LAS char* L, u32 need, int lane) {
  const u32 nd = (need >> (4 * (lane & 7))) & 15u;
#pragma unroll 1
  for (int spins = 0; spins < (1 << 20); ++spins) {
    const u32 v = *(volatile LAS u32*)(L + L_PRG + (lane & 7) * 4);
    if (__builtin_amdgcn_ballot_w64(v < nd) == 0) break;
    __builtin_amdgcn_s_sleep(1);
  }
  asm volatile("" ::: "memory");
}
DI void touch(u32 lds, rsrc_t rs, u32 voff, u32 soff) {
  asm volatile("s_mov_b32 m0, %0\n\ts_nop 0\n\tbuffer_load_dword %1, %2, %3 offen sc1 lds" ::"s"(lds), "v"(voff), "s"(rs), "s"(soff)
               : "memory");
}
#define TS(i)
#define TC(i)
#define TSOUT()

DI void write_part(const f32x16 (&acc)[2], LAS char* L, int slot, int lane) {
#pragma unroll
  for (int ct = 0; ct < 2; ++ct)
#pragma unroll
    for (int i = 0; i < 4; ++i)
      *(LAS f32x4*)(L + (slot * 32 + (lane & 31)) * PROW + (32 * ct + 8 * i + 4 * (lane >> 5)) * 4) =
          f32x4{acc[ct][4 * i], acc[ct][4 * i + 1], acc[ct][4 * i + 2], acc[ct][4 * i + 3]};
}

NOPK __global__ __launch_bounds__(512, 2) void fused_block(const void*, const void*, const void*, const void*, const void*, const void*,
                                                           void*, char*) {
  __shared__ __attribute__((aligned(16))) char smem[LDS_BYTES];
  v8q ka;
  asm volatile("s_load_dwordx16 %0, %1, 0x0" : "=s"(ka) : "s"(__builtin_amdgcn_kernarg_segment_ptr()));
  LAS char* L = (LAS char*)smem;
  const u32 L0 = (u32)(size_t)L;
  const u32 tid = __builtin_amdgcn_workitem_id_x();
  const int b = __builtin_amdgcn_workgroup_id_x(), xcd = b & 7, jj = b >> 3;
  const int lane = tid & 63, w = __builtin_amdgcn_readfirstlane(tid >> 6);
  const int r16 = lane & 15, g = lane >> 4, tq = (lane >> 2) & 3, tp = lane & 3, hh = lane >> 5;
  u32 stampv = 0;
  if (tid >= 448 && tid < 456 + CPF) {
    u64 pc;
    u32 clen;
    asm volatile("s_getpc_b64 %0\n.Lfb_here:\n\ts_mov_b32 %1, .Lfb_last-.Lfb_here" : "=s"(pc), "=s"(clen));
    touch(L0 + L_TCH, mkrs((const void*)pc, clen), (lane < 8 ? jj * 8 + lane : lane - 8) * 128, 0);
  }
  if (tid < 264) *(LAS u32*)(L + (tid < 224 ? L_XH : L_PRG - 896) + tid * 4) = tid < 224 ? 0x3f803f80u : 0u;

  const int tile1 = (xcd >> 1) * 4 + (jj & 3), head = (xcd & 1) * 8 + (jj >> 2);
  const int srow0 = 256 * (tile1 >> 1) + (tile1 & 1);
  const u32 vo1 = (lane >> 2) * 2688 + (lane & 3) * 16;
  const u32 so1 = (w - 5) * 896 + ((56 * head) & ~15), l1 = L0 + (w - 5) * 1024;
  const u32 x_l = L0 + L_X + w * 28672, x_s = (srow0 + 64 * w) * 896;
  const u32 x_v0 = (lane >> 3) * 1792 + ((lane & 7) ^ g) * 16, x_v1 = (8 + (lane >> 3)) * 1792 + ((lane & 7) ^ (4 + g)) * 16;
  if (w < 4) __builtin_amdgcn_s_setprio(2);
  asm volatile("s_waitcnt lgkmcnt(0)\n\ts_barrier" : "+s"(ka) : "s"(x_l), "s"(x_s), "v"(x_v0), "v"(x_v1) : "memory");
  const void *xg = (const void*)ka[0], *wqkv = (const void*)ka[1], *wo = (const void*)ka[2], *wg = (const void*)ka[3];
  const void *wu = (const void*)ka[4], *wd = (const void*)ka[5];
  void* outg = (void*)ka[6];
  char* ws = (char*)ka[7];
  const rsrc_t rsX = mkrs(xg, 2048 * 896);
  if (w < 4) {
    dma_x(x_l, x_v0, x_v1, rsX, x_s);
  } else {
    __builtin_amdgcn_s_sleep(LDELAY);
    if (w > 4 && lane < 32) touch(L0 + L_TCH, mkrs(wqkv, 448 * 2688), (192 + jj * 8 + (lane >> 2)) * 2688 + (w - 5) * 896 + ((xcd & 1) * 3 + (lane & 3)) * 128, 0);
    if (w > 4 || lane < 56)
      dma_loop(w == 4 ? L0 + L_XH + XROW : l1, w == 4 ? lane * 16 : vo1, mkrs(w == 4 ? xg : wqkv, w == 4 ? 2048 * 896 : 448 * 2688),
               w == 4 ? (tile1 < 2 ? 0 : srow0 - 14) * 896 : so1, w == 4 ? XROW : 3072, w == 4 ? 1792 : 16 * 2688, w == 4 ? 6 : 7);
  }
  TS(1);
  TC(48);
  const rsrc_t rsWS = mkrs(ws, WS_END), rsW1 = mkrs(wqkv, 448 * 2688);
  if (w == 4) {
    if (lane < 2) touch(L0 + L_PRG + 64, rsWS, lane * 4, 0);
    const u32 tv = lane < 49 ? b * 6272 + lane * 128 : 0x7fff0000u;
    touch(L0 + L_TCH, mkrs(wo, 448 * 896), lane < 13 ? b * 1664 + lane * 128 : 0x7fff0000u, 0);
    touch(L0 + L_TCH, mkrs(wg, 448 * 3584), tv, 0);
    touch(L0 + L_TCH, mkrs(wu, 448 * 3584), tv, 0);
  }
  const rsrc_t rsWo = mkrs(wo, 448 * 896), rsWg = mkrs(wg, 448 * 3584), rsWu = mkrs(wu, 448 * 3584), rsWd = mkrs(wd, 1792 * 896);
  const bool tail = jj < 28;
  const int tt2 = jj & 3, c2 = jj >> 2;
  const int T3 = (xcd >> 2) * 4 + (jj & 3), fs3 = (xcd & 3) * 7 + (jj >> 2);
  const int stw = w >> 2;
  const u32 vo3 = (lane >> 3) * 4 * 3584 + (lane & 7) * 16;
  const int p2 = tt2 >> 1, q2 = tt2 & 1, rb = w & 1, ch2 = (w >> 1) & 1;
  const int k04 = 28 * (w & 3) + (w < 4 ? 20 : 0);
  const u32 vo4 = (lane >> 3) * 896 + (((lane & 7) ^ ((lane >> 2) & 4)) * 16), so4 = 16 * k04 * 896 + c2 * 128;
  v4u w4f[20][2];
  auto load_w4 = [&](int r) LAMBDA { w4f[r >> 1][r & 1] = __builtin_amdgcn_raw_buffer_load_b128(rsWd, vo4, so4 + r * 8 * 896, 0); };
  const u32 frow = (256 * xcd + 2 * (64 * stw + 32 * q2 + 16 * rb + r16) + p2) * 896 + (tail ? c2 : 0) * 128 + ch2 * 64 + g * 8;
  v2u xra, xrb;
  if (w < 4) {
    LAS char* wa[2];
    wa[0] = L + (8 * hh + tq) * 64 + ((4 * (g & 1) + tp + (head & 1)) & 7) * 8;
    wa[1] = wa[0] + 42 * 1024;
    LAS char* xa[4];
#pragma unroll
    for (int i = 0; i < 4; ++i) xa[i] = L + L_X + w * 28672 + (lane & 31) * 128 + ((2 * i + hh) ^ ((lane >> 1) & 7)) * 16;
    asm volatile("" : "+v"(wa[0]), "+v"(wa[1]), "+v"(xa[0]), "+v"(xa[1]), "+v"(xa[2]), "+v"(xa[3]));
    v4u xf[28];
    v4u mf = {0u, 0u, 0u, 0u};
    {
      float s1[4] = {0.f, 0.f, 0.f, 0.f}, s2[4] = {0.f, 0.f, 0.f, 0.f};
      sfor<0, 8>([&](auto S_) LAMBDA {
        constexpr int sl = decltype(S_)::v;
        if constexpr (sl < 7) {
          waitv<4 * (6 - sl)>();
#pragma unroll
          for (int i = 0; i < 4; ++i) xf[4 * sl + i] = *(LAS v4u*)(xa[i] + sl * 4096);
          if constexpr (sl == 6) {
            prog(L, w, 2);
            TS(2);
          }
          SB();
        }
        if constexpr (sl >= 1) {
#pragma unroll
          for (int ks = 4 * sl - 4; ks < 4 * sl; ++ks)
#pragma unroll
            for (int e = 0; e < 4; ++e) {
              s1[e] = dot2(xf[ks][e], 0x3f803f80u, s1[e]);
              s2[e] = dot2(xf[ks][e], xf[ks][e], s2[e]);
            }
          SB();
        }
      });
      const float nmean = sum32((s1[0] + s1[1]) + (s1[2] + s1[3])) * (-1.f / 448.f), t2 = sum32((s2[0] + s2[1]) + (s2[2] + s2[3]));
      *(LAS float*)(L + L_RS + (8 + 32 * w + (lane & 31)) * 4) =
          __builtin_amdgcn_rsqf(__builtin_fmaxf(t2 * (1.f / 448.f) - nmean * nmean, 0.f) + 1e-5f);
      const u32 hi = pk(nmean, nmean);
      mf[0] = hh ? 0u : hi;
      mf[1] = hh ? 0u : pk(nmean - bflo(hi), 0.f);
      asm volatile("" : "+v"(mf));
    }
    TS(8);
    await(L, 0x11100000u, lane);
    TS(7);
    f32x16 acc[3];
#pragma unroll
    for (int m = 0; m < 3; ++m)
#pragma unroll
      for (int i = 0; i < 16; ++i) acc[m][i] = 0.f;
    constexpr int PD = 3;
    v4u wf[PD + 1];
    auto rdw = [&](int n) LAMBDA {
      LAS char* a = wa[n / 42] + (n % 42) * 1024;
      return trfrag(a, a + 256);
    };
#pragma unroll
    for (int n = 0; n < PD; ++n) wf[n] = rdw(n);
    u32 pv = 0;
    SB();
    sfor<0, 84>([&](auto N_) LAMBDA {
      constexpr int n = decltype(N_)::v;
      if constexpr ((n + PD + 4) % 12 == 0 && n + PD + 4 < 84) pv = *(volatile LAS u32*)(L + L_PRG + (lane & 7) * 4);
      if constexpr ((n + PD) % 12 == 0 && n + PD < 84) {
        if constexpr ((n + PD) % 24 == 0) prog(L, w, 2 + (n + PD) / 24);
        if (__builtin_amdgcn_ballot_w64((lane & 7) >= 5 && pv < (n + PD) / 12 + 1) != 0) await(L, 0x11100000u * ((n + PD) / 12 + 1), lane);
      }
      if constexpr (n == 60) xra = __builtin_amdgcn_raw_buffer_load_b64(rsX, frow, 0, 0);
      if constexpr (n == 66) xrb = __builtin_amdgcn_raw_buffer_load_b64(rsX, frow, 32, 0);
      if constexpr (n + PD < 84) wf[(n + PD) % (PD + 1)] = rdw(n + PD);
      acc[n % 3] = mfma32(wf[n % (PD + 1)], xf[n / 3], acc[n % 3]);
      SB();
    });
    TS(3);
    u32 cs[3];
#pragma unroll 1
    for (int spins = 0; spins < (1 << 20); ++spins) {
      const u32 f = *(volatile LAS u32*)(L + L_PRG + (lane & 7) * 4);
#pragma unroll
      for (int m = 0; m < 3; ++m) cs[m] = *(volatile LAS u32*)(L + L_CS1 + (32 * m + (lane & 31)) * 4);
      if (__builtin_amdgcn_ballot_w64(f < ((0x88803333u >> (4 * (lane & 7))) & 15u)) == 0) break;
      __builtin_amdgcn_s_sleep(1);
    }
#pragma unroll
    for (int m = 0; m < 3; ++m) acc[m] = mfma32(v4u{cs[m], cs[m] & 0xffffu, 0u, 0u}, mf, acc[m]);
#pragma unroll
    for (int m = 0; m < 3; ++m) {
      LAS char* d = L + (m == 0 ? L_Q : (m == 1 ? L_K + 1024 : L_V + 1024)) + (2 * w + (g & 1)) * 1024 + hh * 256 + r16 * 16;
      *(LAS v4u*)d = v4u{pk(acc[m][0], acc[m][1]), pk(acc[m][2], acc[m][3]), pk(acc[m][4], acc[m][5]), pk(acc[m][6], acc[m][7])};
      const u32 p2 = pk(acc[m][12], acc[m][13]), p3 = pk(acc[m][14], acc[m][15]);
      *(LAS v4u*)(d + 512) = v4u{pk(acc[m][8], acc[m][9]), pk(acc[m][10], acc[m][11]), hh ? 0u : p2, hh ? 0u : p3};
    }
  } else {
    LAS char* xb = L + L_XH + (r16 & 7) * XROW + g * 16;
    v4u xh[14];
    if (w == 4) {
      waitv<3 * TOUCH>();
      prog(L, w, 1);
      __builtin_amdgcn_s_setprio(3);
#pragma unroll
      for (int ks = 0; ks < 14; ++ks) xh[ks] = *(LAS v4u*)(xb + ks * 64);
      float s1[4] = {0.f, 0.f, 0.f, 0.f}, s2[4] = {0.f, 0.f, 0.f, 0.f};
#pragma unroll
      for (int ks = 0; ks < 14; ++ks)
#pragma unroll
        for (int e = 0; e < 4; ++e) {
          s1[e] = dot2(xh[ks][e], 0x3f803f80u, s1[e]);
          s2[e] = dot2(xh[ks][e], xh[ks][e], s2[e]);
        }
      const float nm = sum32(sum16((s1[0] + s1[1]) + (s1[2] + s1[3]))) * (-1.f / 448.f), t2 = sum32(sum16((s2[0] + s2[1]) + (s2[2] + s2[3])));
      *(LAS float*)(L + L_PRG + 96 + (lane & 7) * 4) = nm;
      *(LAS float*)(L + L_RS + (lane & 7) * 4) = __builtin_amdgcn_rsqf(__builtin_fmaxf(t2 * (1.f / 448.f) - nm * nm, 0.f) + 1e-5f);
      prog(L, w, 2);
    } else {
      __builtin_amdgcn_s_setprio(3);
      f32x4 h0 = {0.f, 0.f, 0.f, 0.f}, h1 = {0.f, 0.f, 0.f, 0.f};
      LAS char* a0[2];
      LAS char* a1[2];
      a0[0] = L + (3 * (g >> 1) + w - 5) * 1024 + (8 * (g & 1) + tq) * 64 + (tp + (head & 1)) * 8;
      a1[0] = L + (3 * (g >> 1) + w - 5) * 1024 + (8 * (g & 1) + tq) * 64 + ((4 + tp + (head & 1)) & 7) * 8;
      a0[1] = a0[0] + 8 * 6144;
      a1[1] = a1[0] + 8 * 6144;
      asm volatile("" : "+v"(a0[1]), "+v"(a1[1]));
      auto part = [&](auto A_, auto B_) LAMBDA {
        constexpr int A = decltype(A_)::v, B = decltype(B_)::v;
        v4u f[B - A][2];
#pragma unroll
        for (int ks = A; ks < B; ++ks) {
          f[ks - A][0] = trfrag(a0[ks / 8] + (ks % 8) * 6144, a0[ks / 8] + (ks % 8) * 6144 + 256);
          f[ks - A][1] = trfrag(a1[ks / 8] + (ks % 8) * 6144, a1[ks / 8] + (ks % 8) * 6144 + 256);
        }
        SB();
#pragma unroll
        for (int ks = A; ks < B; ++ks) {
          h0 = mfma16(xh[ks], f[ks - A][0], h0);
          h1 = mfma16(xh[ks], f[ks - A][1], h1);
        }
        SB();
      };
      f32x4 hm;
      sfor<0, 7>([&](auto C_) LAMBDA {
        constexpr int c = decltype(C_)::v;
        waitv<(c < 6 ? 4 : 0)>();
        prog(L, w, c + 1);
        if constexpr (c == 1) await(L, 0x00002222u, lane);
        if constexpr (c < 5) dma_run<4, 3072, 16 * 2688>(l1 + (8 + 4 * c) * 3072, vo1, rsW1, so1 + (8 + 4 * c) * 16 * 2688);
        if constexpr (c == 0) {
          await(L, 0x00010000u, lane);
#pragma unroll
          for (int ks = 0; ks < 14; ++ks) xh[ks] = *(LAS v4u*)(xb + ks * 64);
        }
        part(IC<2 * c>{}, IC<2 * c + 2>{});
        if constexpr (c == 5) {
          await(L, 0x22220000u, lane);
          hm = *(LAS f32x4*)(L + L_PRG + 96 + (g & 1) * 16);
        }
      });
      const u32 e0 = pk(h0[0], h0[0]), e1 = pk(h1[0], h1[0]);
      if (g == 0) {
        *(LAS u32*)(L + L_CS1 + ((w - 5) * 32 + r16) * 4) = pk(bflo(e0), h0[0] - bflo(e0));
        *(LAS u32*)(L + L_CS1 + ((w - 5) * 32 + 16 + r16) * 4) = pk(bflo(e1), h1[0] - bflo(e1));
      }
      prog(L, w, 8);
      if (w != 5) {
        auto r0 = __builtin_amdgcn_permlane16_swap(f2u(h0[0]), f2u(h0[0]), false, false);
        auto r1 = __builtin_amdgcn_permlane16_swap(f2u(h1[0]), f2u(h1[0]), false, false);
        const u32 c0u = r0[0], c1u = r1[0];
        LAS char* d = L + (w == 6 ? L_K : L_V) + ((r16 >> 2) & 1) * 256 + (8 + 4 * (g & 1)) * 16 + (r16 >> 3) * 8 + (r16 & 3) * 2;
#pragma unroll
        for (int j = 0; j < 4; ++j) {
          const u32 t = pk(h0[j] + hm[j] * u2f(c0u), r16 < 12 ? h1[j] + hm[j] * u2f(c1u) : 0.f);
          *(LAS unsigned short*)(d + j * 16) = (unsigned short)t;
          *(LAS unsigned short*)(d + j * 16 + 512) = (unsigned short)(t >> 16);
        }
      }
    }
    xra = __builtin_amdgcn_raw_buffer_load_b64(rsX, frow, 0, 0);
    xrb = __builtin_amdgcn_raw_buffer_load_b64(rsX, frow, 32, 0);
    SB();
    if (tail) {
      __builtin_amdgcn_s_setprio(0);
      await(L, 0x77704444u, lane);
      const u32 vo2 = (lane >> 3) * 4 * 896 + (lane & 7) * 16;
      const u32 l2 = L0 + L_W2 + (w - 4) * W2Q, s2 = 112 * (w - 4) * 896 + c2 * 128;
      dma_run<4, W2PL, 896>(l2, vo2, rsWo, s2);
      dma_run<4, W2PL, 896>(l2 + 1024, vo2, rsWo, s2 + 32 * 896);
      dma_run<4, W2PL, 896>(l2 + 2048, vo2, rsWo, s2 + 64 * 896);
      if (lane < 32) dma_run<4, W2PL, 896>(l2 + 3072, vo2, rsWo, s2 + 96 * 896);
      dma_run<7, 1024, 32 * 3584>(L0 + (1 + ch2) * PL + rb * 7168, vo3, rsWg, (1 + ch2 + rb * 224) * 3584 + fs3 * 128);
    }
    TS(3);
  }
  __builtin_amdgcn_s_setprio(0);
  BAR();
  TS(4);
  u64 tag;
  {
    const v4u qf = *(LAS v4u*)(L + L_Q + w * 1024 + lane * 16);
    const v4u kp = *(LAS v4u*)(L + L_K + w * 1024 + lane * 16), kf = *(LAS v4u*)(L + L_K + w * 1024 + 1024 + lane * 16);
    const f32x4 rp = *(LAS f32x4*)(L + L_RS + (16 * w - 8 + 4 * (g | 2)) * 4), ro = *(LAS f32x4*)(L + L_RS + (16 * w + 8 + 4 * g) * 4);
    const float rq = *(LAS float*)(L + L_RS + (16 * w + 8 + r16) * 4) * (0.18898223650461362f * 1.4426950408889634f);
    LAS char* va = L + L_V + w * 1024 + (tp & 1) * 256 + tq * 16 + (tp >> 1) * 8;
    const v4u v0 = trfrag(va + (g | 2) * 64, va + 1024 + g * 64), v1 = trfrag(va + (g | 2) * 64 + 512, va + 1024 + g * 64 + 512);
    const v2u epoch = *(LAS v2u*)(L + L_PRG + 64);
    const f32x4 z = {0.f, 0.f, 0.f, 0.f};
    const f32x4 sp = mfma16(kp, qf, z), so = mfma16(kf, qf, z);
    const bool noprev = (tile1 < 2) && (w == 0);
    float p[8];
    float mx = -3.0e38f;
#pragma unroll
    for (int j = 0; j < 4; ++j) {
      const int d = r16 - (4 * g + j);
      p[j] = (d <= -9 && !noprev) ? sp[j] * rp[j] : -3.0e38f;
      p[4 + j] = (d >= 0 && d <= 7) ? so[j] * ro[j] : -3.0e38f;
      mx = __builtin_fmaxf(mx, __builtin_fmaxf(p[j], p[4 + j]));
    }
    mx = max32(max16(mx));
    float sum = 0.f;
#pragma unroll
    for (int j = 0; j < 8; ++j) {
      p[j] = __builtin_amdgcn_exp2f((p[j] - mx) * rq);
      sum += p[j];
    }
    const v4u pf = {pk(p[0] * rp[0], p[1] * rp[1]), pk(p[2] * rp[2], p[3] * rp[3]), pk(p[4] * ro[0], p[5] * ro[1]), pk(p[6] * ro[2], p[7] * ro[3])};
    const f32x4 o0 = mfma16(v0, pf, z), o1 = mfma16(v1, pf, z);
    sum = sum32(sum16(sum));
    const float inv = __builtin_amdgcn_rcpf(sum);
    const v4u of = {pk(o0[0] * inv, o0[1] * inv), pk(o0[2] * inv, o0[3] * inv), pk(o1[0] * inv, o1[1] * inv), pk(o1[2] * inv, o1[3] * inv)};
    st128(of, rsWS, lane * 16, WS_O + ((tile1 * 8 + w) * 16 + head) * 1024);
    TS(5);
    const u32 lo = __builtin_amdgcn_readfirstlane(epoch[0]), hi = __builtin_amdgcn_readfirstlane(epoch[1]);
    tag = (((u64)hi << 32) | lo) * 6364136223846793005ull + 1442695040888963407ull;
  }
  if (tail && w >= 2) {
    dma_run<7, 1024, 32 * 3584>(L0 + (2 + (w >> 1)) * PL + rb * 7168, vo3, w < 4 ? rsWg : rsWu, ((w < 4 ? 3 : ch2) + rb * 224) * 3584 + fs3 * 128);
    WAITV(7);
  } else {
    WAITV(0);
  }
  asm volatile("s_barrier" ::: "memory");
  if (tid == 0) setflag(ws, FL1 + tile1 * 16 + head, tag);
  TS(6);
  if (!tail) {
#pragma unroll 1
    for (int k = 0; k < 5; ++k) {
      const u32 idx = (k * 8 + w) * 64 + lane;
      touch(L0 + L_TCH, rsWd, idx < 2352 ? ((448 * (jj - 28) + 112) * 7 + idx) * 128 : 0x7fff0000u, 0);
    }
    if (b == 255 && w < 4) {
      poll(ws, FL1 + w * 64 + lane, true, tag);
      if (lane == 0) {
        u32 old = __hip_atomic_fetch_add((LAS u32*)(L + L_PRG + 32), 1u, RLX_WG);
        if (old == 3) __hip_atomic_store((GAS u64*)ws, tag, RLX_AGENT);
      }
    }
    TSOUT();
    return;
  }

  v4u hres;
  {
    const int blk = (xcd * 2 + p2) * 8 + 4 * stw + 2 * q2 + rb;
    v4u of[8], w2f[8][4];
    auto read_w2 = [&]() LAMBDA {
      LAS char* a = L + L_W2 + 2 * ch2 * W2Q + tq * W2PL + g * 128 + tp * 8;
      LAS char* b0[2] = {a + ch2 * 64, a + (ch2 ^ 1) * 64};
      LAS char* b1[2] = {b0[0] + (g < 3 ? 512 : 0), b0[1] + (g < 3 ? 512 : 0)};
#pragma unroll
      for (int hd = 0; hd < 8; ++hd)
#pragma unroll
        for (int cb = 0; cb < 4; ++cb) {
          const u32 o = (hd >> 2) * W2Q + (hd & 3) * 896 + (cb & 1) * 32;
          w2f[hd][cb] = trfrag(b0[cb >> 1] + o, b1[cb >> 1] + o);
        }
    };
    if (w >= 4) {
#pragma unroll
      for (int r = 0; r < W4H; ++r) load_w4(r);
      SB();
    } else if (w >= 2) {
      dma_run<7, 1024, 32 * 3584>(L0 + rb * 7168, vo3, rsWg, rb * 224 * 3584 + fs3 * 128);
    }
    read_w2();
    TS(16);
    if (w == 0) {
      __builtin_amdgcn_s_sleep(PD1);
      poll(ws, FL1 + (xcd * 2 + p2) * 16 + lane, lane < 16, tag);
    }
    BAR();
    TS(17);
#pragma unroll
    for (int hd = 0; hd < 8; ++hd)
      of[hd] = __builtin_amdgcn_raw_buffer_load_b128(rsWS, lane * 16, WS_O + (blk * 16 + 8 * ch2 + hd) * 1024, 16);
    SB();
    f32x4 a0 = {bflo(xra[0]), bfhi(xra[0]), bflo(xra[1]), bfhi(xra[1])}, a1 = {bflo(xrb[0]), bfhi(xrb[0]), bflo(xrb[1]), bfhi(xrb[1])};
    f32x4 a2 = {0.f, 0.f, 0.f, 0.f}, a3 = {0.f, 0.f, 0.f, 0.f};
#pragma unroll
    for (int hd = 0; hd < 8; ++hd) {
      a2 = mfma16(w2f[hd][2], of[hd], a2);
      a3 = mfma16(w2f[hd][3], of[hd], a3);
      a0 = mfma16(w2f[hd][0], of[hd], a0);
      a1 = mfma16(w2f[hd][1], of[hd], a1);
    }
    *(LAS f32x4*)(L + L_S4 + w * 2048 + lane * 16) = a2;
    *(LAS f32x4*)(L + L_S4 + w * 2048 + 1024 + lane * 16) = a3;
    BAR();
    a0 += *(LAS f32x4*)(L + L_S4 + (w ^ 2) * 2048 + lane * 16);
    a1 += *(LAS f32x4*)(L + L_S4 + (w ^ 2) * 2048 + 1024 + lane * 16);
    TS(18);
    hres = v4u{pk(a0[0], a0[1]), pk(a0[2], a0[3]), pk(a1[0], a1[1]), pk(a1[2], a1[3])};
    const int t32 = (xcd * 2 + stw) * 4 + tt2;
    st128(hres, rsWS, (g >> 1) * 1024 + ((g & 1) * 32 + 16 * rb + r16) * 16, WS_H + (t32 * 28 + 4 * c2 + 2 * ch2) * 1024);
    float s1 = 0.f, s2 = 0.f;
#pragma unroll
    for (int i = 0; i < 4; ++i) {
      const float lo = bflo(hres[i]), hi = bfhi(hres[i]);
      s1 += lo + hi;
      s2 += lo * lo + hi * hi;
    }
    s1 = sum32(sum16(s1));
    s2 = sum32(sum16(s2));
    if (lane < 16)
      __builtin_amdgcn_raw_buffer_store_b64(v2u{f2u(s1), f2u(s2)}, rsWS, (16 * rb + r16) * 8, WS_ST + (t32 * 14 + 2 * c2 + ch2) * 256, 16);
    TS(19);
    asm volatile("s_waitcnt vmcnt(0)\n\ts_barrier" ::: "memory");
    if (tid == 0) setflag(ws, FL2 + xcd * 28 + jj, tag);
    TS(20);
  }
  const u32 lb = w < 4 ? L_S4 + w * 8192 : L_T4 + (w - 4) * 2048;
  LAS char* ta0 = L + (4 * hh + tq) * 128 + (tq >> 1) * 64 + (g & 1) * 32 + tp * 8;
  LAS char* ta1 = L + (4 * hh + tq) * 128 + ((tq >> 1) ^ 1) * 64 + (g & 1) * 32 + tp * 8;
  auto tr_w4 = [&](int k) LAMBDA {
    asm volatile("" ::: "memory");
    *(LAS v4u*)(L + lb + lane * 16) = w4f[k][0];
    *(LAS v4u*)(L + lb + 1024 + lane * 16) = w4f[k][1];
    asm volatile("" ::: "memory");
    w4f[k][0] = trfrag(ta0 + lb, ta0 + lb + 1024);
    w4f[k][1] = trfrag(ta1 + lb, ta1 + lb + 1024);
  };
  if (w < 4) {
    dma_run<7, 1024, 32 * 3584>(L0 + (6 + ch2) * PL + rb * 7168, vo3, rsWu, (2 + ch2 + rb * 224) * 3584 + fs3 * 128);
    WAITV(0);
    prog(L, w, 11);
    __builtin_amdgcn_s_setprio(3);
    await(L, 0x0000BBBBu, lane);
    {
      f32x4 cg = {0.f, 0.f, 0.f, 0.f}, cu = {0.f, 0.f, 0.f, 0.f};
      const v4u ones = {0x3f803f80u, 0x3f803f80u, 0x3f803f80u, 0x3f803f80u};
      LAS char* ag = L + tq * PL + g * 128 + tp * 8 + w * 32;
      LAS char* au = ag + 4 * PL;
      asm volatile("" : "+v"(au));
      v4u f[6];
      sfor<0, 34>([&](auto N_) LAMBDA {
        constexpr int n = decltype(N_)::v;
        if constexpr (n >= 6) {
          if constexpr (n & 1) cu = mfma16(ones, f[n % 6], cu);
          else cg = mfma16(ones, f[n % 6], cg);
        }
        if constexpr (n < 28) {
          LAS char* a = ((n & 1) ? au : ag) + (n >> 1) * 1024;
          f[n % 6] = trfrag(a, a + 512);
        }
        SB();
      });
      const float c = (g & 1) ? cu[0] : cg[0];
      const u32 hi = pk(c, c);
      *(LAS u32*)(L + L_CS3 + ((lane & 31) * 4 + w) * 4) = pk(bflo(hi), c - bflo(hi));
      prog(L, w, 12);
    }
    __builtin_amdgcn_s_setprio(1);
    await(L, 0xBBBB0000u, lane);
    dma_run<8, 1024, 8 * 896>(L0 + lb, vo4, rsWd, so4);
  } else {
    __builtin_amdgcn_s_setprio(2);
#pragma unroll
    for (int k = 0; k < TRE; ++k) tr_w4(k);
    if (w == 4) {
      poll(ws, FL2 + T3 * 28 + lane, lane < 28, tag);
      post(L, 2);
    } else {
      pend(L, 2);
    }
  }

  {
    TS(24);
    TC(49);
    const int t32 = T3 * 8 + w;
    LAS char* a3 = L + (4 * (g & 1) + tq) * PL + hh * 128 + tp * 8;
    constexpr int PD = 4, HD = 14;
    v4u hb[HD], sv[4];
#pragma unroll
    for (int ks = 0; ks < HD; ++ks) hb[ks] = __builtin_amdgcn_raw_buffer_load_b128(rsWS, lane * 16, WS_H + (t32 * 28 + ks) * 1024, 16);
    SB();
    if (w >= 4) {
      prog(L, w, 11);
#pragma unroll
      for (int k = TRE; k < W4H / 2; ++k) tr_w4(k);
      await(L, 0x0000BBBBu, lane);
    }
    TS(21);
    f32x16 acc[4];
#pragma unroll
    for (int c = 0; c < 4; ++c)
#pragma unroll
      for (int i = 0; i < 16; ++i) acc[c][i] = 0.f;
    v4u wf[PD + 1], csv;
    auto rdw = [&](int n) LAMBDA {
      const int ks = n >> 2, c = n & 3;
      if (ks == 28) return v4u{csv[c], csv[c] & 0xffffu, 0u, 0u};
      const u32 o = (ks >> 1) * 1024 + (ks & 1) * 256 + c * 32;
      return trfrag(a3 + o, a3 + o + 512);
    };
#pragma unroll
    for (int n = 0; n < PD; ++n) wf[n] = rdw(n);
    float s1 = 0.f, s2 = 0.f, ng = 0.f, irs = 0.f, st4[4];
    v4u mf = {0u, 0u, 0u, 0u};
    u32 pv = 0;
    SB();
    sfor<0, 116>([&](auto N_) LAMBDA {
      constexpr int n = decltype(N_)::v, S0 = 56, ks = n >> 2, c = n & 3;
      if (n == 4) TC(50);
      if (n == 58) TC(51);
      if constexpr (n == 92) pv = *(volatile LAS u32*)(L + L_PRG + (lane & 3) * 4);
      if constexpr (n == 97)
        if (__builtin_amdgcn_ballot_w64(pv < 12) != 0) await(L, 0x0000CCCCu, lane);
      if constexpr (n == 100) csv = *(LAS v4u*)(L + L_CS3 + (lane & 31) * 16);
      if constexpr (n + PD < 116) wf[(n + PD) % (PD + 1)] = rdw(n + PD);
      if constexpr (ks == 28) acc[c] = mfma32(wf[n % (PD + 1)], mf, acc[c]);
      else acc[c] = mfma32(wf[n % (PD + 1)], hb[ks % HD], acc[c]);
      if constexpr (c == 3 && ks + HD < 28)
        hb[ks % HD] = __builtin_amdgcn_raw_buffer_load_b128(rsWS, lane * 16, WS_H + (t32 * 28 + ks + HD) * 1024, 16);
      if constexpr (c == 1 && ks < 4)
        sv[ks] = __builtin_amdgcn_raw_buffer_load_b128(rsWS, (ks == 3 && hh) ? 0x7fff0000u : lane * 16, WS_ST + t32 * 14 * 256 + ks * 1024, 16);
      if constexpr (n >= S0 && n < S0 + 8 && !(n & 1)) {
        constexpr int e = (n - S0) / 2;
        st4[e] = sum32(sum16((u2f(sv[0][e]) + u2f(sv[1][e])) + (u2f(sv[2][e]) + u2f(sv[3][e]))));
      }
      if constexpr (n == S0 + 8) {
#pragma unroll
        for (int e = 0; e < 4; ++e) st4[e] = bperm((lane & 31) >> 1, st4[e]);
      }
      if constexpr (n == S0 + 13) {
        s1 = ((lane & 1) ? st4[2] : st4[0]) * (-1.f / 448.f);
        s2 = (lane & 1) ? st4[3] : st4[1];
      }
      if constexpr (n == S0 + 15) {
        const u32 hi = pk(s1, s1);
        mf[0] = hh ? 0u : hi;
        mf[1] = hh ? 0u : pk(s1 - bflo(hi), 0.f);
      }
      if constexpr (n == S0 + 17) {
        const float var = __builtin_fmaxf(s2 * (1.f / 448.f) - s1 * s1, 0.f) + 1e-5f;
        const float rstd = __builtin_amdgcn_rsqf(var);
        ng = -rstd * 1.4426950408889634f;
        irs = var * rstd;
      }
      SB();
    });
    prog(L, w, 13);
    TS(25);
    TC(52);
    if (w >= 4) {
#pragma unroll
      for (int r = W4H; r < 30; ++r) load_w4(r);
      SB();
    }
#pragma unroll
    for (int c = 0; c < 4; ++c) {
      float o[8];
#pragma unroll
      for (int i = 0; i < 8; ++i) o[i] = acc[c][8 + i] * __builtin_amdgcn_rcpf(__builtin_amdgcn_exp2f(acc[c][i] * ng) * irs + irs);
      st128(v4u{pk(o[0], o[1]), pk(o[2], o[3]), pk(o[4], o[5]), pk(o[6], o[7])}, rsWS, lane * 16, WS_HH + (t32 * 112 + fs3 * 4 + c) * 1024);
    }
    TS(26);
    TC(53);
    if (w < 4) {
#pragma unroll
      for (int k = 0; k < 4; ++k) {
        w4f[k][0] = trfrag(ta0 + lb + k * 2048, ta0 + lb + k * 2048 + 1024);
        w4f[k][1] = trfrag(ta1 + lb + k * 2048, ta1 + lb + k * 2048 + 1024);
      }
    }
    WAITV(0);
    arrive_flag(L, 1 + stw, 4, ws, FL3 + (T3 * 2 + stw) * 28 + fs3, tag, lane);
    prog(L, w, 14);
    TS(27);
  }
  auto phase4 = [&](auto N_) LAMBDA {
    constexpr int N = decltype(N_)::v, D = N < 9 ? N : 9;
#pragma unroll 1
    for (int st = 1; st >= 0; --st) {
      if (w == 4 * st) {
        poll(ws, FL3 + (xcd * 2 + st) * 28 + lane, lane < 28, tag);
        post(L, st);
      } else {
        pend(L, st);
      }
      TS(33 - 4 * st);
      const u32 hb4 = WS_HH + ((xcd * 8 + 4 * st + tt2) * 112 + k04) * 1024;
      v4u hf[D];
#pragma unroll
      for (int kk = 0; kk < D; ++kk) hf[kk] = __builtin_amdgcn_raw_buffer_load_b128(rsWS, lane * 16, hb4 + kk * 1024, 16);
      SB();
      if (N == 8 && st) {
        waitv<8>();
#pragma unroll
        for (int k = 4; k < 8; ++k) {
          w4f[k][0] = trfrag(ta0 + lb + (k - 4) * 2048, ta0 + lb + (k - 4) * 2048 + 1024);
          w4f[k][1] = trfrag(ta1 + lb + (k - 4) * 2048, ta1 + lb + (k - 4) * 2048 + 1024);
        }
        prog(L, w, 15);
      }
      f32x16 ac4[2];
#pragma unroll
      for (int i = 0; i < 16; ++i) ac4[0][i] = ac4[1][i] = 0.f;
#pragma unroll
      for (int kk = 0; kk < N; ++kk) {
        const v4u cur = hf[kk % D];
        ac4[0] = mfma32(w4f[kk][0], cur, ac4[0]);
        ac4[1] = mfma32(w4f[kk][1], cur, ac4[1]);
        if (kk + D < N) hf[kk % D] = __builtin_amdgcn_raw_buffer_load_b128(rsWS, lane * 16, hb4 + (kk + D) * 1024, 16);
        SB();
      }
      TS(34 - 4 * st);
      await(L, st ? 0xDDDDDDDDu : 0x0000FFFFu, lane);
      write_part(ac4, L, (1 - st) * 8 + w, lane);
      TS(35 - 4 * st);
    }
  };
  if (w >= 4) {
    __builtin_amdgcn_s_setprio(0);
#pragma unroll
    for (int r = 30; r < 40; ++r) load_w4(r);
    SB();
#pragma unroll
    for (int k = W4H / 2; k < 20; ++k) tr_w4(k);
    TS(28);
    phase4(IC<20>{});
  } else {
    WAITL();
    dma_run<8, 1024, 8 * 896>(L0 + lb, vo4, rsWd, so4 + 4 * 16 * 896);
    phase4(IC<8>{});
  }
  BAR();
  TS(38);
  {
    f32x4 lo = {bflo(hres[0]), bfhi(hres[0]), bflo(hres[1]), bfhi(hres[1])}, hi = {bflo(hres[2]), bfhi(hres[2]), bflo(hres[3]), bfhi(hres[3])};
    const int l2 = __builtin_amdgcn_mbcnt_hi(~0u, __builtin_amdgcn_mbcnt_lo(~0u, 0u)), r2 = l2 & 15, g2 = l2 >> 4;
    const u32 frow2 = (256 * xcd + 2 * (64 * stw + 32 * q2 + 16 * rb + r2) + p2) * 896 + c2 * 128 + ch2 * 64 + g2 * 8;
    LAS char* pa = L + ((1 - stw) * 8 * 32 + 16 * rb + r2) * PROW + (32 * ch2 + 4 * g2) * 4;
#pragma unroll
    for (int k = 0; k < 8; ++k) {
      lo += *(LAS f32x4*)(pa + k * 32 * PROW);
      hi += *(LAS f32x4*)(pa + k * 32 * PROW + 64);
    }
    *(GAS v2u*)((GAS char*)outg + frow2) = v2u{pk(lo[0], lo[1]), pk(lo[2], lo[3])};
    *(GAS v2u*)((GAS char*)outg + frow2 + 32) = v2u{pk(hi[0], hi[1]), pk(hi[2], hi[3])};
    asm volatile(".Lfb_last:");
    TS(39);
    TC(54);
  }
  TSOUT();
}

extern "C" void kernel_launch(void* const* d_in, const int* in_sizes, int n_in, void* d_out, int out_size, void* d_ws,
                              size_t ws_size, hipStream_t stream) {
  fused_block<<<256, 512, 0, stream>>>(d_in[0], d_in[1], d_in[2], d_in[4], d_in[5], d_in[6], d_out, (char*)d_ws);
}
```

```cpp
#include <hip/hip_runtime.h>
#include <hip/hip_bf16.h>

typedef unsigned u32;
typedef unsigned long long u64;
typedef u32 v4u __attribute__((ext_vector_type(4)));
typedef u32 v2u __attribute__((ext_vector_type(2)));
typedef u64 v8q __attribute__((ext_vector_type(8)));
typedef short s16x4 __attribute__((ext_vector_type(4)));
typedef short s16x8 __attribute__((ext_vector_type(8)));
typedef __bf16 bf16x2 __attribute__((ext_vector_type(2)));
typedef __bf16 bf16x8 __attribute__((ext_vector_type(8)));
typedef float f32x2 __attribute__((ext_vector_type(2)));
typedef float f32x4 __attribute__((ext_vector_type(4)));
typedef float f32x16 __attribute__((ext_vector_type(16)));
#define GAS __attribute__((address_space(1)))
#define LAS __attribute__((address_space(3)))
#if defined(__HIP_DEVICE_COMPILE__)
#define NOPK __attribute__((target("no-packed-fp32-ops")))
#else
#define NOPK
#endif
#define DI __device__ __forceinline__ NOPK
typedef __amdgpu_buffer_rsrc_t rsrc_t;
template <int V>
struct IC {
  static constexpr int v = V;
};
template <int A, int B, class F>
DI void sfor(F&& f) {
  if constexpr (B - A == 1) {
    f(IC<A>{});
  } else if constexpr (B - A > 1) {
    sfor<A, (A + B) / 2>(f);
    sfor<(A + B) / 2, B>(f);
  }
}
#define LAMBDA __attribute__((always_inline)) NOPK

#define XB_DELAY 12
#define W3EARLY 10
#define PSLEEP 4
#define PD1 4
#define TRE 4
#define CPF 24
#define PD2 4
#define W4T 8
#define W4H 14
#define PD3 4
#define TS1 40
#define TS2 60
#define LDELAY 14
#define TOUCH1 1
#define TOUCH 1
#define STAMP 0

constexpr u32 WS_FLAG = 4096;
constexpr u32 WS_O = 1024 * 1024;
constexpr u32 WS_H = WS_O + 2 * 1024 * 1024;
constexpr u32 WS_ST = WS_H + 64 * 28 * 1024;
constexpr u32 WS_HH = WS_ST + 256 * 1024;
constexpr u32 WS_END = WS_HH + 64 * 112 * 1024;
constexpr u32 WS_DBG = 16 * 1024 * 1024;
constexpr int FL1 = 0, FL2 = 256, FL3 = 768;

constexpr u32 XROW = 912;
constexpr u32 L_X = 36864;
constexpr u32 L_XH = 153600;
constexpr u32 PL = 14368;
constexpr u32 L_W2 = 6 * PL;
constexpr u32 W2PL = 3616, W2Q = 4 * W2PL;
constexpr u32 L_Q = 0;
constexpr u32 L_K = L_W2 + 4 * W2Q;
constexpr u32 L_V = L_K + 9 * 1024;
constexpr u32 L_RS = L_V + 9 * 1024;
constexpr u32 L_CS1 = L_RS + 544;
constexpr u32 L_TCH = L_CS1 + 384;
constexpr u32 L_PRG = L_TCH + 256;
constexpr u32 L_S4 = 8 * PL;
constexpr u32 L_T4 = L_S4 + 32768;
constexpr u32 L_CS3 = L_RS;
constexpr u32 PROW = 272;
constexpr u32 LDS_BYTES = 163840;
constexpr int NA4 = 16, NB4 = 12;

#define WAITV_(n) asm volatile("s_waitcnt vmcnt(" #n ")" ::: "memory")
#define WAITV(n) WAITV_(n)
#define BAR() asm volatile("s_waitcnt lgkmcnt(0)\n\ts_barrier" ::: "memory")
#define SB() __builtin_amdgcn_sched_barrier(0)

DI void dma16(u32 lds, u32 voff, rsrc_t rs, u32 soff) {
  asm volatile("s_mov_b32 m0, %0\n\ts_nop 0\n\tbuffer_load_dwordx4 %1, %2, %3 offen lds" ::"s"(lds), "v"(voff), "s"(rs), "s"(soff)
               : "memory");
}
template <int N, int DL, int DS>
DI void dma_run(u32 lds, u32 voff, rsrc_t rs, u32 soff) {
  u32 t;
  asm volatile(
      "s_mov_b32 m0, %1\n\ts_mov_b32 %0, %4\n\t"
      ".rept %c5\n\tbuffer_load_dwordx4 %2, %3, %0 offen lds\n\ts_add_u32 m0, m0, %c6\n\ts_add_u32 %0, %0, %c7\n\t.endr"
      : "=&s"(t)
      : "s"(lds), "v"(voff), "s"(rs), "s"(soff), "i"(N), "i"(DL), "i"(DS)
      : "memory", "scc");
}
template <int N>
DI void dma_run_s(u32 lds, u32 voff, rsrc_t rs, u32 soff, u32 dl, u32 ds) {
  u32 t;
  asm volatile(
      "s_mov_b32 m0, %1\n\ts_mov_b32 %0, %4\n\t"
      ".rept %c5\n\tbuffer_load_dwordx4 %2, %3, %0 offen lds\n\ts_add_u32 m0, m0, %6\n\ts_add_u32 %0, %0, %7\n\t.endr"
      : "=&s"(t)
      : "s"(lds), "v"(voff), "s"(rs), "s"(soff), "i"(N), "s"(dl), "s"(ds)
      : "memory", "scc");
}
DI void dma_loop(u32 lds, u32 voff, rsrc_t rs, u32 soff, u32 dl, u32 ds, u32 nm1) {
  asm volatile(
      "s_mov_b32 m0, %2\n"
      "1:\n\ts_sub_u32 %1, %1, 1\n\tbuffer_load_dwordx4 %3, %4, %0 offen lds\n\ts_cbranch_scc1 2f\n\t"
      "s_add_u32 m0, m0, %5\n\ts_add_u32 %0, %0, %6\n\ts_branch 1b\n2:"
      : "+s"(soff), "+s"(nm1)
      : "s"(lds), "v"(voff), "s"(rs), "s"(dl), "s"(ds)
      : "memory", "scc");
}
DI void dma_x(u32 lds, u32 v0, u32 v1, rsrc_t rs, u32 soff) {
  u32 n, t;
  asm volatile(
      "s_mov_b32 m0, %3\n\ts_mov_b32 %1, 6\n"
      "1:\n\ts_add_u32 %2, %0, 28672\n\tbuffer_load_dwordx4 %4, %6, %0 offen lds\n\ts_add_u32 m0, m0, 1024\n\ts_nop 0\n\t"
      "buffer_load_dwordx4 %5, %6, %0 offen lds\n\ts_add_u32 m0, m0, 1024\n\ts_add_u32 %0, %0, 128\n\t"
      "buffer_load_dwordx4 %4, %6, %2 offen lds\n\ts_add_u32 m0, m0, 1024\n\ts_nop 0\n\t"
      "buffer_load_dwordx4 %5, %6, %2 offen lds\n\ts_add_u32 m0, m0, 1024\n\ts_sub_u32 %1, %1, 1\n\ts_cbranch_scc0 1b"
      : "+s"(soff), "=&s"(n), "=&s"(t)
      : "s"(lds), "v"(v0), "v"(v1), "s"(rs)
      : "memory", "scc");
}
template <int N>
DI void waitv() { asm volatile("s_waitcnt vmcnt(%c0)" ::"i"(N) : "memory"); }
DI void st128(v4u v, rsrc_t rs, u32 voff, u32 soff) {
  __builtin_amdgcn_raw_buffer_store_b128(v, rs, voff, soff, 16);
  asm volatile("s_nop 1" ::"v"(v) : "memory");
}
DI rsrc_t mkrs(const void* p, u32 bytes) { return __builtin_amdgcn_make_buffer_rsrc((void*)p, (short)0, (int)bytes, 0x00020000); }
DI v4u trfrag(LAS char* a, LAS char* b) {
  s16x4 lo = __builtin_amdgcn_ds_read_tr16_b64_v4i16((LAS s16x4*)a);
  s16x4 hi = __builtin_amdgcn_ds_read_tr16_b64_v4i16((LAS s16x4*)b);
  return __builtin_bit_cast(v4u, __builtin_shufflevector(lo, hi, 0, 1, 2, 3, 4, 5, 6, 7));
}
DI f32x4 mfma16(v4u a, v4u b, f32x4 c) {
  return __builtin_amdgcn_mfma_f32_16x16x32_bf16(__builtin_bit_cast(bf16x8, a), __builtin_bit_cast(bf16x8, b), c, 0, 0, 0);
}
DI f32x16 mfma32(v4u a, v4u b, f32x16 c) {
  return __builtin_amdgcn_mfma_f32_32x32x16_bf16(__builtin_bit_cast(bf16x8, a), __builtin_bit_cast(bf16x8, b), c, 0, 0, 0);
}
DI float u2f(u32 u) { return __builtin_bit_cast(float, u); }
DI u32 f2u(float f) { return __builtin_bit_cast(u32, f); }
DI u32 pk(float a, float b) {
  f32x2 f = {a, b};
  return __builtin_bit_cast(u32, __builtin_convertvector(f, bf16x2));
}
DI float bflo(u32 u) { return __builtin_bit_cast(float, u << 16); }
DI float bfhi(u32 u) { return __builtin_bit_cast(float, u & 0xffff0000u); }
DI float dot2(u32 a, u32 b, float c) {
  return __builtin_amdgcn_fdot2_f32_bf16(__builtin_bit_cast(bf16x2, a), __builtin_bit_cast(bf16x2, b), c, false);
}
DI float sum16(float x) {
  auto r = __builtin_amdgcn_permlane16_swap(__builtin_bit_cast(u32, x), __builtin_bit_cast(u32, x), false, false);
  const u32 a = r[0], c = r[1];
  return u2f(a) + u2f(c);
}
DI float sum32(float x) {
  auto r = __builtin_amdgcn_permlane32_swap(__builtin_bit_cast(u32, x), __builtin_bit_cast(u32, x), false, false);
  const u32 a = r[0], c = r[1];
  return u2f(a) + u2f(c);
}
DI float max16(float x) {
  auto r = __builtin_amdgcn_permlane16_swap(__builtin_bit_cast(u32, x), __builtin_bit_cast(u32, x), false, false);
  const u32 a = r[0], c = r[1];
  float m;
  asm("v_max_f32 %0, %1, %2" : "=v"(m) : "v"(a), "v"(c));
  return m;
}
DI float max32(float x) {
  auto r = __builtin_amdgcn_permlane32_swap(__builtin_bit_cast(u32, x), __builtin_bit_cast(u32, x), false, false);
  const u32 a = r[0], c = r[1];
  float m;
  asm("v_max_f32 %0, %1, %2" : "=v"(m) : "v"(a), "v"(c));
  return m;
}
DI float bperm(int srclane, float x) {
  return __builtin_bit_cast(float, __builtin_amdgcn_ds_bpermute(srclane * 4, __builtin_bit_cast(int, x)));
}
#define RLX_AGENT __ATOMIC_RELAXED, __HIP_MEMORY_SCOPE_AGENT
#define RLX_WG __ATOMIC_RELAXED, __HIP_MEMORY_SCOPE_WORKGROUP
#define WAITL() asm volatile("s_waitcnt lgkmcnt(0)" ::: "memory")
DI void poll(char* ws, int idx, bool act, u64 tag) {
  GAS u64* p = (GAS u64*)(ws + WS_FLAG + (u32)idx * 128u);
#pragma unroll 1
  for (int spins = 0; spins < (1 << 16); ++spins) {
    if (act) act = __hip_atomic_load(p, RLX_AGENT) != tag;
    if (__builtin_amdgcn_ballot_w64(act) == 0) break;
    __builtin_amdgcn_s_sleep(PSLEEP);
  }
  asm volatile("" ::: "memory");
}
DI void setflag(char* ws, int idx, u64 tag) { __hip_atomic_store((GAS u64*)(ws + WS_FLAG + (u32)idx * 128u), tag, RLX_AGENT); }
DI void arrive_flag(LAS char* L, int cnt, u32 n, char* ws, int idx, u64 tag, int lane) {
  if (lane == 0) {
    const u32 old = __hip_atomic_fetch_add((LAS u32*)(L + L_PRG + 32 + cnt * 4), 1u, RLX_WG);
    if (old == n - 1) __hip_atomic_store((GAS u64*)(ws + WS_FLAG + (u32)idx * 128u), tag, RLX_AGENT);
  }
}
DI void post(LAS char* L, int k) {
  asm volatile("" ::: "memory");
  *(volatile LAS u32*)(L + L_PRG + 128 + k * 4) = 1u;
  SB();
}
DI void pend(LAS char* L, int k) {
#pragma unroll 1
  for (int spins = 0; spins < (1 << 22); ++spins) {
    if (__builtin_amdgcn_readfirstlane(*(volatile LAS u32*)(L + L_PRG + 128 + k * 4)) != 0) break;
    __builtin_amdgcn_s_sleep(1);
  }
  asm volatile("" ::: "memory");
}
DI void prog(LAS char* L, int w, u32 stage) {
  asm volatile("" ::: "memory");
  *(volatile LAS u32*)(L + L_PRG + w * 4) = stage;
  SB();
}
DI void prog2(LAS char* L, int w, u32 stage) {
  asm volatile("" ::: "memory");
  *(volatile LAS u32*)(L + L_PRG + 96 + w * 4) = stage;
  SB();
}
DI void await2(LAS char* L, u32 stage, int lane) {
#pragma unroll 1
  for (int spins = 0; spins < (1 << 20); ++spins) {
    const u32 v = *(volatile LAS u32*)(L + L_PRG + 96 + (lane & 7) * 4);
    if (__builtin_amdgcn_ballot_w64(v < stage) == 0) break;
    __builtin_amdgcn_s_sleep(1);
  }
  asm volatile("" ::: "memory");
}
DI void await(LAS char* L, u32 need, int lane) {
  const u32 nd = (need >> (4 * (lane & 7))) & 15u;
#pragma unroll 1
  for (int spins = 0; spins < (1 << 20); ++spins) {
    const u32 v = *(volatile LAS u32*)(L + L_PRG + (lane & 7) * 4);
    if (__builtin_amdgcn_ballot_w64(v < nd) == 0) break;
    __builtin_amdgcn_s_sleep(1);
  }
  asm volatile("" ::: "memory");
}
DI void touch(u32 lds, rsrc_t rs, u32 voff, u32 soff) {
  asm volatile("s_mov_b32 m0, %0\n\ts_nop 0\n\tbuffer_load_dword %1, %2, %3 offen sc1 lds" ::"s"(lds), "v"(voff), "s"(rs), "s"(soff)
               : "memory");
}
#define TS(i)
#define TC(i)
#define TSOUT()

DI void write_part(const f32x16 (&acc)[2], LAS char* L, int slot, int lane) {
#pragma unroll
  for (int ct = 0; ct < 2; ++ct)
#pragma unroll
    for (int i = 0; i < 4; ++i)
      *(LAS f32x4*)(L + (slot * 32 + (lane & 31)) * PROW + (32 * ct + 8 * i + 4 * (lane >> 5)) * 4) =
          f32x4{acc[ct][4 * i], acc[ct][4 * i + 1], acc[ct][4 * i + 2], acc[ct][4 * i + 3]};
}

NOPK __global__ __launch_bounds__(512, 2) void fused_block(const void*, const void*, const void*, const void*, const void*, const void*,
                                                           void*, char*) {
  __shared__ __attribute__((aligned(16))) char smem[LDS_BYTES];
  v8q ka;
  asm volatile("s_load_dwordx16 %0, %1, 0x0" : "=s"(ka) : "s"(__builtin_amdgcn_kernarg_segment_ptr()));
  LAS char* L = (LAS char*)smem;
  const u32 L0 = (u32)(size_t)L;
  const u32 tid = __builtin_amdgcn_workitem_id_x();
  const int b = __builtin_amdgcn_workgroup_id_x(), xcd = b & 7, jj = b >> 3;
  const int lane = tid & 63, w = __builtin_amdgcn_readfirstlane(tid >> 6);
  const int r16 = lane & 15, g = lane >> 4, tq = (lane >> 2) & 3, tp = lane & 3, hh = lane >> 5;
  u32 stampv = 0;
  if (tid >= 448 && tid < 456 + CPF) {
    u64 pc;
    u32 clen;
    asm volatile("s_getpc_b64 %0\n.Lfb_here:\n\ts_mov_b32 %1, .Lfb_last-.Lfb_here" : "=s"(pc), "=s"(clen));
    touch(L0 + L_TCH, mkrs((const void*)pc, clen), (lane < 8 ? jj * 8 + lane : lane - 8) * 128, 0);
  }
  if (tid < 264) *(LAS u32*)(L + (tid < 224 ? L_XH : L_PRG - 896) + tid * 4) = tid < 224 ? 0x3f803f80u : 0u;

  const int tile1 = (xcd >> 1) * 4 + (jj & 3), head = (xcd & 1) * 8 + (jj >> 2);
  const int srow0 = 256 * (tile1 >> 1) + (tile1 & 1);
  const u32 vo1 = (lane >> 2) * 2688 + (lane & 3) * 16;
  const u32 so1 = (w - 5) * 896 + ((56 * head) & ~15), l1 = L0 + (w - 5) * 1024;
  const u32 x_l = L0 + L_X + w * 28672, x_s = (srow0 + 64 * w) * 896;
  const u32 x_v0 = (lane >> 3) * 1792 + ((lane & 7) ^ g) * 16, x_v1 = (8 + (lane >> 3)) * 1792 + ((lane & 7) ^ (4 + g)) * 16;
  if (w < 4) __builtin_amdgcn_s_setprio(2);
  asm volatile("s_waitcnt lgkmcnt(0)\n\ts_barrier" : "+s"(ka) : "s"(x_l), "s"(x_s), "v"(x_v0), "v"(x_v1) : "memory");
  const void *xg = (const void*)ka[0], *wqkv = (const void*)ka[1], *wo = (const void*)ka[2], *wg = (const void*)ka[3];
  const void *wu = (const void*)ka[4], *wd = (const void*)ka[5];
  void* outg = (void*)ka[6];
  char* ws = (char*)ka[7];
  const rsrc_t rsX = mkrs(xg, 2048 * 896);
  if (w < 4) {
    dma_x(x_l, x_v0, x_v1, rsX, x_s);
  } else {
    __builtin_amdgcn_s_sleep(LDELAY);
    if (w > 4 && lane < 32) touch(L0 + L_TCH, mkrs(wqkv, 448 * 2688), (192 + jj * 8 + (lane >> 2)) * 2688 + (w - 5) * 896 + ((xcd & 1) * 3 + (lane & 3)) * 128, 0);
    if (w > 4 || lane < 56)
      dma_loop(w == 4 ? L0 + L_XH + XROW : l1, w == 4 ? lane * 16 : vo1, mkrs(w == 4 ? xg : wqkv, w == 4 ? 2048 * 896 : 448 * 2688),
               w == 4 ? (tile1 < 2 ? 0 : srow0 - 14) * 896 : so1, w == 4 ? XROW : 3072, w == 4 ? 1792 : 16 * 2688, w == 4 ? 6 : 7);
  }
  TS(1);
  TC(48);
  const rsrc_t rsWS = mkrs(ws, WS_END), rsW1 = mkrs(wqkv, 448 * 2688);
  if (w == 4) {
    if (lane < 2) touch(L0 + L_PRG + 64, rsWS, lane * 4, 0);
    const u32 tv = lane < 49 ? b * 6272 + lane * 128 : 0x7fff0000u;
    touch(L0 + L_TCH, mkrs(wo, 448 * 896), lane < 13 ? b * 1664 + lane * 128 : 0x7fff0000u, 0);
    touch(L0 + L_TCH, mkrs(wg, 448 * 3584), tv, 0);
    touch(L0 + L_TCH, mkrs(wu, 448 * 3584), tv, 0);
  }
  const rsrc_t rsWo = mkrs(wo, 448 * 896), rsWg = mkrs(wg, 448 * 3584), rsWu = mkrs(wu, 448 * 3584), rsWd = mkrs(wd, 1792 * 896);
  const bool tail = jj < 28;
  const int tt2 = jj & 3, c2 = jj >> 2;
  const int T3 = (xcd >> 2) * 4 + (jj & 3), fs3 = (xcd & 3) * 7 + (jj >> 2);
  const int stw = w >> 2;
  const u32 vo3 = (lane >> 3) * 4 * 3584 + (lane & 7) * 16;
  const int p2 = tt2 >> 1, q2 = tt2 & 1, rb = w & 1, ch2 = (w >> 1) & 1;
  const int k04 = 28 * (w & 3) + (w < 4 ? 14 : 0);
  const u32 vo4 = (lane >> 3) * 896 + (((lane & 7) ^ ((lane >> 2) & 4)) * 16), so4 = 16 * k04 * 896 + c2 * 128;
  v4u w4f[14][2];
  auto load_w4 = [&](int r) LAMBDA { w4f[r >> 1][r & 1] = __builtin_amdgcn_raw_buffer_load_b128(rsWd, vo4, so4 + r * 8 * 896, 0); };
  const u32 frow = (256 * xcd + 2 * (64 * stw + 32 * q2 + 16 * rb + r16) + p2) * 896 + (tail ? c2 : 0) * 128 + ch2 * 64 + g * 8;
  v2u xra, xrb;
  if (w < 4) {
    LAS char* wa[2];
    wa[0] = L + (8 * hh + tq) * 64 + ((4 * (g & 1) + tp + (head & 1)) & 7) * 8;
    wa[1] = wa[0] + 42 * 1024;
    LAS char* xa[4];
#pragma unroll
    for (int i = 0; i < 4; ++i) xa[i] = L + L_X + w * 28672 + (lane & 31) * 128 + ((2 * i + hh) ^ ((lane >> 1) & 7)) * 16;
    asm volatile("" : "+v"(wa[0]), "+v"(wa[1]), "+v"(xa[0]), "+v"(xa[1]), "+v"(xa[2]), "+v"(xa[3]));
    v4u xf[28];
    v4u mf = {0u, 0u, 0u, 0u};
    {
      float s1[4] = {0.f, 0.f, 0.f, 0.f}, s2[4] = {0.f, 0.f, 0.f, 0.f};
      sfor<0, 8>([&](auto S_) LAMBDA {
        constexpr int sl = decltype(S_)::v;
        if constexpr (sl < 7) {
          waitv<4 * (6 - sl)>();
#pragma unroll
          for (int i = 0; i < 4; ++i) xf[4 * sl + i] = *(LAS v4u*)(xa[i] + sl * 4096);
          if constexpr (sl == 6) {
            prog(L, w, 2);
            TS(2);
          }
          SB();
        }
        if constexpr (sl >= 1) {
#pragma unroll
          for (int ks = 4 * sl - 4; ks < 4 * sl; ++ks)
#pragma unroll
            for (int e = 0; e < 4; ++e) {
              s1[e] = dot2(xf[ks][e], 0x3f803f80u, s1[e]);
              s2[e] = dot2(xf[ks][e], xf[ks][e], s2[e]);
            }
          SB();
        }
      });
      const float nmean = sum32((s1[0] + s1[1]) + (s1[2] + s1[3])) * (-1.f / 448.f), t2 = sum32((s2[0] + s2[1]) + (s2[2] + s2[3]));
      *(LAS float*)(L + L_RS + (8 + 32 * w + (lane & 31)) * 4) =
          __builtin_amdgcn_rsqf(__builtin_fmaxf(t2 * (1.f / 448.f) - nmean * nmean, 0.f) + 1e-5f);
      const u32 hi = pk(nmean, nmean);
      mf[0] = hh ? 0u : hi;
      mf[1] = hh ? 0u : pk(nmean - bflo(hi), 0.f);
      asm volatile("" : "+v"(mf));
    }
    TS(8);
    await(L, 0x11100000u, lane);
    TS(7);
    f32x16 acc[3];
#pragma unroll
    for (int m = 0; m < 3; ++m)
#pragma unroll
      for (int i = 0; i < 16; ++i) acc[m][i] = 0.f;
    constexpr int PD = 7;
    v4u wf[PD + 1];
    auto rdw = [&](int n) LAMBDA {
      LAS char* a = wa[n / 42] + (n % 42) * 1024;
      return trfrag(a, a + 256);
    };
#pragma unroll
    for (int n = 0; n < PD; ++n) wf[n] = rdw(n);
    u32 pv = 0;
    SB();
    sfor<0, 84>([&](auto N_) LAMBDA {
      constexpr int n = decltype(N_)::v;
      if constexpr ((n + PD + 4) % 12 == 0 && n + PD + 4 < 84) pv = *(volatile LAS u32*)(L + L_PRG + (lane & 7) * 4);
      if constexpr ((n + PD) % 12 == 0 && n + PD < 84) {
        if constexpr ((n + PD) % 24 == 0) prog(L, w, 2 + (n + PD) / 24);
        if (__builtin_amdgcn_ballot_w64((lane & 7) >= 5 && pv < (n + PD) / 12 + 1) != 0) await(L, 0x11100000u * ((n + PD) / 12 + 1), lane);
      }
      if constexpr (n == 60) xra = __builtin_amdgcn_raw_buffer_load_b64(rsX, frow, 0, 0);
      if constexpr (n == 66) xrb = __builtin_amdgcn_raw_buffer_load_b64(rsX, frow, 32, 0);
      if constexpr (n + PD < 84) wf[(n + PD) % (PD + 1)] = rdw(n + PD);
      acc[n % 3] = mfma32(wf[n % (PD + 1)], xf[n / 3], acc[n % 3]);
      SB();
    });
    TS(3);
    u32 cs[3];
#pragma unroll 1
    for (int spins = 0; spins < (1 << 20); ++spins) {
      const u32 f = *(volatile LAS u32*)(L + L_PRG + (lane & 7) * 4);
#pragma unroll
      for (int m = 0; m < 3; ++m) cs[m] = *(volatile LAS u32*)(L + L_CS1 + (32 * m + (lane & 31)) * 4);
      if (__builtin_amdgcn_ballot_w64(f < ((0x88803333u >> (4 * (lane & 7))) & 15u)) == 0) break;
      __builtin_amdgcn_s_sleep(1);
    }
#pragma unroll
    for (int m = 0; m < 3; ++m) acc[m] = mfma32(v4u{cs[m], cs[m] & 0xffffu, 0u, 0u}, mf, acc[m]);
#pragma unroll
    for (int m = 0; m < 3; ++m) {
      LAS char* d = L + (m == 0 ? L_Q : (m == 1 ? L_K + 1024 : L_V + 1024)) + (2 * w + (g & 1)) * 1024 + hh * 256 + r16 * 16;
      *(LAS v4u*)d = v4u{pk(acc[m][0], acc[m][1]), pk(acc[m][2], acc[m][3]), pk(acc[m][4], acc[m][5]), pk(acc[m][6], acc[m][7])};
      const u32 p2 = pk(acc[m][12], acc[m][13]), p3 = pk(acc[m][14], acc[m][15]);
      *(LAS v4u*)(d + 512) = v4u{pk(acc[m][8], acc[m][9]), pk(acc[m][10], acc[m][11]), hh ? 0u : p2, hh ? 0u : p3};
    }
  } else {
    LAS char* xb = L + L_XH + (r16 & 7) * XROW + g * 16;
    v4u xh[14];
    if (w == 4) {
      waitv<3 * TOUCH>();
      prog(L, w, 1);
      __builtin_amdgcn_s_setprio(3);
#pragma unroll
      for (int ks = 0; ks < 14; ++ks) xh[ks] = *(LAS v4u*)(xb + ks * 64);
      float s1[4] = {0.f, 0.f, 0.f, 0.f}, s2[4] = {0.f, 0.f, 0.f, 0.f};
#pragma unroll
      for (int ks = 0; ks < 14; ++ks)
#pragma unroll
        for (int e = 0; e < 4; ++e) {
          s1[e] = dot2(xh[ks][e], 0x3f803f80u, s1[e]);
          s2[e] = dot2(xh[ks][e], xh[ks][e], s2[e]);
        }
      const float nm = sum32(sum16((s1[0] + s1[1]) + (s1[2] + s1[3]))) * (-1.f / 448.f), t2 = sum32(sum16((s2[0] + s2[1]) + (s2[2] + s2[3])));
      *(LAS float*)(L + L_PRG + 96 + (lane & 7) * 4) = nm;
      *(LAS float*)(L + L_RS + (lane & 7) * 4) = __builtin_amdgcn_rsqf(__builtin_fmaxf(t2 * (1.f / 448.f) - nm * nm, 0.f) + 1e-5f);
      prog(L, w, 2);
    } else {
      __builtin_amdgcn_s_setprio(3);
      f32x4 h0 = {0.f, 0.f, 0.f, 0.f}, h1 = {0.f, 0.f, 0.f, 0.f};
      LAS char* a0[2];
      LAS char* a1[2];
      a0[0] = L + (3 * (g >> 1) + w - 5) * 1024 + (8 * (g & 1) + tq) * 64 + (tp + (head & 1)) * 8;
      a1[0] = L + (3 * (g >> 1) + w - 5) * 1024 + (8 * (g & 1) + tq) * 64 + ((4 + tp + (head & 1)) & 7) * 8;
      a0[1] = a0[0] + 8 * 6144;
      a1[1] = a1[0] + 8 * 6144;
      asm volatile("" : "+v"(a0[1]), "+v"(a1[1]));
      auto part = [&](auto A_, auto B_) LAMBDA {
        constexpr int A = decltype(A_)::v, B = decltype(B_)::v;
        v4u f[B - A][2];
#pragma unroll
        for (int ks = A; ks < B; ++ks) {
          f[ks - A][0] = trfrag(a0[ks / 8] + (ks % 8) * 6144, a0[ks / 8] + (ks % 8) * 6144 + 256);
          f[ks - A][1] = trfrag(a1[ks / 8] + (ks % 8) * 6144, a1[ks / 8] + (ks % 8) * 6144 + 256);
        }
        SB();
#pragma unroll
        for (int ks = A; ks < B; ++ks) {
          h0 = mfma16(xh[ks], f[ks - A][0], h0);
          h1 = mfma16(xh[ks], f[ks - A][1], h1);
        }
        SB();
      };
      f32x4 hm;
      sfor<0, 7>([&](auto C_) LAMBDA {
        constexpr int c = decltype(C_)::v;
        waitv<(c < 6 ? 4 : 0)>();
        prog(L, w, c + 1);
        if constexpr (c == 1) await(L, 0x00002222u, lane);
        if constexpr (c < 5) dma_run<4, 3072, 16 * 2688>(l1 + (8 + 4 * c) * 3072, vo1, rsW1, so1 + (8 + 4 * c) * 16 * 2688);
        if constexpr (c == 0) {
          await(L, 0x00010000u, lane);
#pragma unroll
          for (int ks = 0; ks < 14; ++ks) xh[ks] = *(LAS v4u*)(xb + ks * 64);
        }
        part(IC<2 * c>{}, IC<2 * c + 2>{});
        if constexpr (c == 5) {
          await(L, 0x22220000u, lane);
          hm = *(LAS f32x4*)(L + L_PRG + 96 + (g & 1) * 16);
        }
      });
      const u32 e0 = pk(h0[0], h0[0]), e1 = pk(h1[0], h1[0]);
      if (g == 0) {
        *(LAS u32*)(L + L_CS1 + ((w - 5) * 32 + r16) * 4) = pk(bflo(e0), h0[0] - bflo(e0));
        *(LAS u32*)(L + L_CS1 + ((w - 5) * 32 + 16 + r16) * 4) = pk(bflo(e1), h1[0] - bflo(e1));
      }
      prog(L, w, 8);
      if (w != 5) {
        auto r0 = __builtin_amdgcn_permlane16_swap(f2u(h0[0]), f2u(h0[0]), false, false);
        auto r1 = __builtin_amdgcn_permlane16_swap(f2u(h1[0]), f2u(h1[0]), false, false);
        const u32 c0u = r0[0], c1u = r1[0];
        LAS char* d = L + (w == 6 ? L_K : L_V) + ((r16 >> 2) & 1) * 256 + (8 + 4 * (g & 1)) * 16 + (r16 >> 3) * 8 + (r16 & 3) * 2;
#pragma unroll
        for (int j = 0; j < 4; ++j) {
          const u32 t = pk(h0[j] + hm[j] * u2f(c0u), r16 < 12 ? h1[j] + hm[j] * u2f(c1u) : 0.f);
          *(LAS unsigned short*)(d + j * 16) = (unsigned short)t;
          *(LAS unsigned short*)(d + j * 16 + 512) = (unsigned short)(t >> 16);
        }
      }
    }
    xra = __builtin_amdgcn_raw_buffer_load_b64(rsX, frow, 0, 0);
    xrb = __builtin_amdgcn_raw_buffer_load_b64(rsX, frow, 32, 0);
    SB();
    if (tail) {
      __builtin_amdgcn_s_setprio(0);
      await(L, 0x77704444u, lane);
      const u32 vo2 = (lane >> 3) * 4 * 896 + (lane & 7) * 16;
      const u32 l2 = L0 + L_W2 + (w - 4) * W2Q, s2 = 112 * (w - 4) * 896 + c2 * 128;
      dma_run<4, W2PL, 896>(l2, vo2, rsWo, s2);
      dma_run<4, W2PL, 896>(l2 + 1024, vo2, rsWo, s2 + 32 * 896);
      dma_run<4, W2PL, 896>(l2 + 2048, vo2, rsWo, s2 + 64 * 896);
      if (lane < 32) dma_run<4, W2PL, 896>(l2 + 3072, vo2, rsWo, s2 + 96 * 896);
      dma_run<7, 1024, 32 * 3584>(L0 + (1 + ch2) * PL + rb * 7168, vo3, rsWg, (1 + ch2 + rb * 224) * 3584 + fs3 * 128);
    }
    TS(3);
  }
  __builtin_amdgcn_s_setprio(0);
  u64 tag;
  {
    const bool noprev = (tile1 < 2) && (w == 0);
    bool pr[4], ok[4];
#pragma unroll
    for (int j = 0; j < 4; ++j) {
      const int d = r16 - (4 * g + j);
      pr[j] = d <= -9;
      ok[j] = pr[j] ? !noprev : (u32)d <= 7u;
    }
    LAS char* qa = L + L_Q + w * 1024 + lane * 16;
    LAS char* ka = L + L_K + w * 1024 + lane * 16;
    LAS char* va = L + L_V + w * 1024 + (tp & 1) * 256 + tq * 16 + (tp >> 1) * 8;
    LAS char *vp = va + (g | 2) * 64, *vo = va + 1024 + g * 64;
    LAS char *ra = L + L_RS + (16 * w - 8 + 4 * (g | 2)) * 4, *rb2 = L + L_RS + (16 * w + 8 + 4 * g) * 4, *rc = L + L_RS + (16 * w + 8 + r16) * 4;
    const u32 oso = WS_O + ((tile1 * 8 + w) * 16 + head) * 1024;
    asm volatile("" : "+v"(qa), "+v"(ka), "+v"(vp), "+v"(vo), "+v"(ra), "+v"(rb2), "+v"(rc) : "s"(oso));
    BAR();
    TS(4);
    const v4u qf = *(LAS v4u*)qa, kp = *(LAS v4u*)ka, kf = *(LAS v4u*)(ka + 1024);
    const f32x4 rp = *(LAS f32x4*)ra, ro = *(LAS f32x4*)rb2;
    const float rq = *(LAS float*)rc * (0.18898223650461362f * 1.4426950408889634f);
    const v4u v0 = trfrag(vp, vo), v1 = trfrag(vp + 512, vo + 512);
    const v2u epoch = *(LAS v2u*)(L + L_PRG + 64);
    SB();
    const f32x4 z = {0.f, 0.f, 0.f, 0.f};
    const f32x4 sp = mfma16(kp, qf, z), so = mfma16(kf, qf, z);
    float p[4], rk[4];
#pragma unroll
    for (int j = 0; j < 4; ++j) {
      rk[j] = pr[j] ? rp[j] : ro[j];
      p[j] = ok[j] ? (pr[j] ? sp[j] : so[j]) * (rk[j] * rq) : -3.0e38f;
    }
    const float mx = max32(max16(__builtin_fmaxf(__builtin_fmaxf(p[0], p[1]), __builtin_fmaxf(p[2], p[3]))));
#pragma unroll
    for (int j = 0; j < 4; ++j) p[j] = __builtin_amdgcn_exp2f(p[j] - mx);
    float sum = (p[0] + p[1]) + (p[2] + p[3]);
#pragma unroll
    for (int j = 0; j < 4; ++j) p[j] *= rk[j];
    const v4u pf = {pk(pr[0] ? p[0] : 0.f, pr[1] ? p[1] : 0.f), pk(pr[2] ? p[2] : 0.f, pr[3] ? p[3] : 0.f),
                    pk(pr[0] ? 0.f : p[0], pr[1] ? 0.f : p[1]), pk(pr[2] ? 0.f : p[2], pr[3] ? 0.f : p[3])};
    const f32x4 o0 = mfma16(v0, pf, z), o1 = mfma16(v1, pf, z);
    sum = sum32(sum16(sum));
    const float inv = __builtin_amdgcn_rcpf(sum);
    const v4u of = {pk(o0[0] * inv, o0[1] * inv), pk(o0[2] * inv, o0[3] * inv), pk(o1[0] * inv, o1[1] * inv), pk(o1[2] * inv, o1[3] * inv)};
    st128(of, rsWS, lane * 16, oso);
    TS(5);
    const u32 lo = __builtin_amdgcn_readfirstlane(epoch[0]), hi = __builtin_amdgcn_readfirstlane(epoch[1]);
    tag = (((u64)hi << 32) | lo) * 6364136223846793005ull + 1442695040888963407ull;
  }
  if (tail && w >= 2) {
    dma_run<7, 1024, 32 * 3584>(L0 + (2 + (w >> 1)) * PL + rb * 7168, vo3, w < 4 ? rsWg : rsWu, ((w < 4 ? 3 : ch2) + rb * 224) * 3584 + fs3 * 128);
    WAITV(7);
  } else {
    WAITV(0);
  }
  asm volatile("s_barrier" ::: "memory");
  if (tid == 0) setflag(ws, FL1 + tile1 * 16 + head, tag);
  TS(6);
  if (!tail) {
#pragma unroll 1
    for (int k = 0; k < 5; ++k) {
      const u32 idx = (k * 8 + w) * 64 + lane;
      touch(L0 + L_TCH, rsWd, idx < 2352 ? ((448 * (jj - 28) + 112) * 7 + idx) * 128 : 0x7fff0000u, 0);
    }
    if (b == 255 && w < 4) {
      poll(ws, FL1 + w * 64 + lane, true, tag);
      if (lane == 0) {
        u32 old = __hip_atomic_fetch_add((LAS u32*)(L + L_PRG + 32), 1u, RLX_WG);
        if (old == 3) __hip_atomic_store((GAS u64*)ws, tag, RLX_AGENT);
      }
    }
    TSOUT();
    return;
  }

  v4u hres;
  {
    const int blk = (xcd * 2 + p2) * 8 + 4 * stw + 2 * q2 + rb;
    v4u of[8], w2f[8][4];
    auto read_w2 = [&]() LAMBDA {
      LAS char* a = L + L_W2 + 2 * ch2 * W2Q + tq * W2PL + g * 128 + tp * 8;
      LAS char* b0[2] = {a + ch2 * 64, a + (ch2 ^ 1) * 64};
      LAS char* b1[2] = {b0[0] + (g < 3 ? 512 : 0), b0[1] + (g < 3 ? 512 : 0)};
#pragma unroll
      for (int hd = 0; hd < 8; ++hd)
#pragma unroll
        for (int cb = 0; cb < 4; ++cb) {
          const u32 o = (hd >> 2) * W2Q + (hd & 3) * 896 + (cb & 1) * 32;
          w2f[hd][cb] = trfrag(b0[cb >> 1] + o, b1[cb >> 1] + o);
        }
    };
    if (w >= 4) {
#pragma unroll
      for (int r = 0; r < W4H; ++r) load_w4(r);
      SB();
    } else if (w >= 2) {
      dma_run<7, 1024, 32 * 3584>(L0 + rb * 7168, vo3, rsWg, rb * 224 * 3584 + fs3 * 128);
    }
    read_w2();
    TS(16);
    if (w == 0) {
      __builtin_amdgcn_s_sleep(PD1);
      poll(ws, FL1 + (xcd * 2 + p2) * 16 + lane, lane < 16, tag);
    }
    BAR();
    TS(17);
#pragma unroll
    for (int hd = 0; hd < 8; ++hd)
      of[hd] = __builtin_amdgcn_raw_buffer_load_b128(rsWS, lane * 16, WS_O + (blk * 16 + 8 * ch2 + hd) * 1024, 16);
    SB();
    f32x4 a0 = {bflo(xra[0]), bfhi(xra[0]), bflo(xra[1]), bfhi(xra[1])}, a1 = {bflo(xrb[0]), bfhi(xrb[0]), bflo(xrb[1]), bfhi(xrb[1])};
    f32x4 a2 = {0.f, 0.f, 0.f, 0.f}, a3 = {0.f, 0.f, 0.f, 0.f};
#pragma unroll
    for (int hd = 0; hd < 8; ++hd) {
      a2 = mfma16(w2f[hd][2], of[hd], a2);
      a3 = mfma16(w2f[hd][3], of[hd], a3);
      a0 = mfma16(w2f[hd][0], of[hd], a0);
      a1 = mfma16(w2f[hd][1], of[hd], a1);
    }
    *(LAS f32x4*)(L + L_S4 + w * 2048 + lane * 16) = a2;
    *(LAS f32x4*)(L + L_S4 + w * 2048 + 1024 + lane * 16) = a3;
    BAR();
    a0 += *(LAS f32x4*)(L + L_S4 + (w ^ 2) * 2048 + lane * 16);
    a1 += *(LAS f32x4*)(L + L_S4 + (w ^ 2) * 2048 + 1024 + lane * 16);
    TS(18);
    hres = v4u{pk(a0[0], a0[1]), pk(a0[2], a0[3]), pk(a1[0], a1[1]), pk(a1[2], a1[3])};
    const int t32 = (xcd * 2 + stw) * 4 + tt2;
    st128(hres, rsWS, (g >> 1) * 1024 + ((g & 1) * 32 + 16 * rb + r16) * 16, WS_H + (t32 * 28 + 4 * c2 + 2 * ch2) * 1024);
    float s1 = 0.f, s2 = 0.f;
#pragma unroll
    for (int i = 0; i < 4; ++i) {
      const float lo = bflo(hres[i]), hi = bfhi(hres[i]);
      s1 += lo + hi;
      s2 += lo * lo + hi * hi;
    }
    s1 = sum32(sum16(s1));
    s2 = sum32(sum16(s2));
    if (lane < 16)
      __builtin_amdgcn_raw_buffer_store_b64(v2u{f2u(s1), f2u(s2)}, rsWS, (16 * rb + r16) * 8, WS_ST + (t32 * 14 + 2 * c2 + ch2) * 256, 16);
    prog2(L, w, 0);
    TS(19);
    asm volatile("s_waitcnt vmcnt(0)\n\ts_barrier" ::: "memory");
    if (tid == 0) setflag(ws, FL2 + xcd * 28 + jj, tag);
    TS(20);
  }
  const u32 lb = w < 4 ? L_S4 + w * 8192 : L_T4 + (w - 4) * 2048;
  LAS char* ta0 = L + (4 * hh + tq) * 128 + (tq >> 1) * 64 + (g & 1) * 32 + tp * 8;
  LAS char* ta1 = L + (4 * hh + tq) * 128 + ((tq >> 1) ^ 1) * 64 + (g & 1) * 32 + tp * 8;
  auto tr_w4 = [&](int k) LAMBDA {
    asm volatile("" ::: "memory");
    *(LAS v4u*)(L + lb + lane * 16) = w4f[k][0];
    *(LAS v4u*)(L + lb + 1024 + lane * 16) = w4f[k][1];
    asm volatile("" ::: "memory");
    w4f[k][0] = trfrag(ta0 + lb, ta0 + lb + 1024);
    w4f[k][1] = trfrag(ta1 + lb, ta1 + lb + 1024);
  };
  auto rd4 = [&](int k, u32 o, u32 d) LAMBDA {
    w4f[k][0] = trfrag(ta0 + o, ta0 + o + d);
    w4f[k][1] = trfrag(ta1 + o, ta1 + o + d);
  };
  if (w < 4) {
    dma_run<7, 1024, 32 * 3584>(L0 + (6 + ch2) * PL + rb * 7168, vo3, rsWu, (2 + ch2 + rb * 224) * 3584 + fs3 * 128);
    WAITV(0);
    prog(L, w, 11);
    __builtin_amdgcn_s_setprio(3);
    await(L, 0x0000BBBBu, lane);
    {
      f32x4 cg = {0.f, 0.f, 0.f, 0.f}, cu = {0.f, 0.f, 0.f, 0.f};
      const v4u ones = {0x3f803f80u, 0x3f803f80u, 0x3f803f80u, 0x3f803f80u};
      LAS char* ag = L + tq * PL + g * 128 + tp * 8 + w * 32;
      LAS char* au = ag + 4 * PL;
      asm volatile("" : "+v"(au));
      v4u f[6];
      sfor<0, 34>([&](auto N_) LAMBDA {
        constexpr int n = decltype(N_)::v;
        if constexpr (n >= 6) {
          if constexpr (n & 1) cu = mfma16(ones, f[n % 6], cu);
          else cg = mfma16(ones, f[n % 6], cg);
        }
        if constexpr (n < 28) {
          LAS char* a = ((n & 1) ? au : ag) + (n >> 1) * 1024;
          f[n % 6] = trfrag(a, a + 512);
        }
        SB();
      });
      const float c = (g & 1) ? cu[0] : cg[0];
      const u32 hi = pk(c, c);
      *(LAS u32*)(L + L_CS3 + ((lane & 31) * 4 + w) * 4) = pk(bflo(hi), c - bflo(hi));
      prog(L, w, 12);
    }
    __builtin_amdgcn_s_setprio(1);
    await(L, 0xBBBB0000u, lane);
    dma_run<8, 1024, 8 * 896>(L0 + lb, vo4, rsWd, so4);
    sfor<0, 11>([&](auto K_) LAMBDA {
      constexpr int k = decltype(K_)::v;
      if constexpr (k < 10) {
        waitv<(k == 0 ? 6 : 4)>();
        rd4(k, lb + (k & 3) * 2048, 1024);
      }
      if constexpr (k >= 1) {
        if constexpr (k < 10) asm volatile("s_waitcnt lgkmcnt(4)" ::: "memory");
        else WAITL();
        dma_run<2, 1024, 8 * 896>(L0 + lb + ((k - 1) & 3) * 2048, vo4, rsWd, so4 + (k + 3) * 16 * 896);
      }
    });
    TS(22);
    await(L, 0xCCCC0000u, lane);
  } else {
    __builtin_amdgcn_s_setprio(2);
#pragma unroll
    for (int k = 0; k < TRE; ++k) tr_w4(k);
    if (w == 4) {
      poll(ws, FL2 + T3 * 28 + lane, lane < 28, tag);
      post(L, 2);
    } else {
      pend(L, 2);
    }
  }

  v4u hf[14];
  const u32 hb1 = WS_HH + ((xcd * 8 + 4 + tt2) * 112 + k04) * 1024;
  {
    TS(24);
    TC(49);
    const int t32 = T3 * 8 + w;
    LAS char* a3 = L + (4 * (g & 1) + tq) * PL + hh * 128 + tp * 8;
    constexpr int PD = 4, HD = 12, H0 = 10;
    v4u hb[HD], sv[4];
#pragma unroll
    for (int ks = 0; ks < H0; ++ks) hb[ks] = __builtin_amdgcn_raw_buffer_load_b128(rsWS, lane * 16, WS_H + (t32 * 28 + ks) * 1024, 16);
    SB();
    if (w >= 4) {
      prog(L, w, 11);
#pragma unroll
      for (int k = TRE; k < W4H / 2; ++k) tr_w4(k);
      await(L, 0x0000BBBBu, lane);
    } else {
      await(L, 13u << (4 * (w + 4)), lane);
    }
    TS(21);
    f32x16 acc[4];
#pragma unroll
    for (int c = 0; c < 4; ++c)
#pragma unroll
      for (int i = 0; i < 16; ++i) acc[c][i] = 0.f;
    v4u wf[PD + 1], csv;
    auto rdw = [&](int n) LAMBDA {
      const int ks = n >> 2, c = n & 3;
      if (ks == 28) return v4u{csv[c], csv[c] & 0xffffu, 0u, 0u};
      const u32 o = (ks >> 1) * 1024 + (ks & 1) * 256 + c * 32;
      return trfrag(a3 + o, a3 + o + 512);
    };
#pragma unroll
    for (int n = 0; n < PD; ++n) wf[n] = rdw(n);
    float s1 = 0.f, s2 = 0.f, ng = 0.f, irs = 0.f, st4[4];
    v4u mf = {0u, 0u, 0u, 0u};
    u32 pv = 0;
    SB();
    sfor<0, 116>([&](auto N_) LAMBDA {
      constexpr int n = decltype(N_)::v, S0 = 72, ks = n >> 2, c = n & 3;
      if (n == 4) TC(50);
      if (n == 58) TC(51);
      if constexpr (n + PD == 56) prog2(L, w, 1);
      if constexpr (n == TS1) prog(L, w, 12);
      if constexpr (n == TS2) prog(L, w, 13);
      if constexpr (n == 92) pv = *(volatile LAS u32*)(L + L_PRG + (lane & 3) * 4);
      if constexpr (n == 97)
        if (__builtin_amdgcn_ballot_w64(pv < 12) != 0) await(L, 0x0000CCCCu, lane);
      if constexpr (n == 100) csv = *(LAS v4u*)(L + L_CS3 + (lane & 31) * 16);
      if constexpr (n + PD < 116) wf[(n + PD) % (PD + 1)] = rdw(n + PD);
      if constexpr (ks == 28) acc[c] = mfma32(wf[n % (PD + 1)], mf, acc[c]);
      else acc[c] = mfma32(wf[n % (PD + 1)], hb[ks % HD], acc[c]);
      if constexpr (c == 3 && H0 + ks < 28)
        hb[(H0 + ks) % HD] = __builtin_amdgcn_raw_buffer_load_b128(rsWS, lane * 16, WS_H + (t32 * 28 + H0 + ks) * 1024, 16);
      if constexpr (n % 6 == 0 && n >= 12 && n < 36)
        sv[n / 6 - 2] = __builtin_amdgcn_raw_buffer_load_b128(rsWS, (n == 30 && hh) ? 0x7fff0000u : lane * 16,
                                                              WS_ST + t32 * 14 * 256 + (n / 6 - 2) * 1024, 16);
      if constexpr (n >= S0 && n < S0 + 4) st4[n - S0] = (u2f(sv[0][n - S0]) + u2f(sv[1][n - S0])) + (u2f(sv[2][n - S0]) + u2f(sv[3][n - S0]));
      if constexpr (n >= S0 + 4 && n < S0 + 8) st4[n - S0 - 4] = sum16(st4[n - S0 - 4]);
      if constexpr (n >= S0 + 8 && n < S0 + 12) st4[n - S0 - 8] = sum32(st4[n - S0 - 8]);
      if constexpr (n >= S0 + 12 && n < S0 + 16) st4[n - S0 - 12] = bperm((lane & 31) >> 1, st4[n - S0 - 12]);
      if constexpr (n == S0 + 20) {
        const float ta = st4[2], tb = st4[0];
        s1 = ((lane & 1) ? ta : tb) * (-1.f / 448.f);
      }
      if constexpr (n == S0 + 21) {
        const float ta = st4[3], tb = st4[1];
        s2 = ((lane & 1) ? ta : tb) * (1.f / 448.f);
      }
      if constexpr (n == S0 + 22) mf[0] = pk(s1, s1);
      if constexpr (n == S0 + 23) mf[1] = pk(s1 - bflo(mf[0]), 0.f);
      if constexpr (n == S0 + 24) {
        mf[0] = hh ? 0u : mf[0];
        mf[1] = hh ? 0u : mf[1];
      }
      if constexpr (n == S0 + 25) s2 = __builtin_fmaxf(s2 - s1 * s1, 0.f) + 1e-5f;
      if constexpr (n == S0 + 26) s1 = __builtin_amdgcn_rsqf(s2);
      if constexpr (n == S0 + 27) {
        ng = -s1 * 1.4426950408889634f;
        irs = s2 * s1;
      }
      SB();
    });
    TS(25);
    TC(52);
#pragma unroll
    for (int c = 0; c < 4; ++c) {
      float o[8];
#pragma unroll
      for (int i = 0; i < 8; ++i) o[i] = acc[c][8 + i] * __builtin_amdgcn_rcpf(__builtin_amdgcn_exp2f(acc[c][i] * ng) * irs + irs);
      st128(v4u{pk(o[0], o[1]), pk(o[2], o[3]), pk(o[4], o[5]), pk(o[6], o[7])}, rsWS, lane * 16, WS_HH + (t32 * 112 + fs3 * 4 + c) * 1024);
    }
    TS(26);
    TC(53);
    if (w < 4) {
#pragma unroll
      for (int k = 10; k < 14; ++k) rd4(k, lb + (k & 3) * 2048, 1024);
      pend(L, 1);
#pragma unroll
      for (int kk = 0; kk < 14; ++kk) hf[kk] = __builtin_amdgcn_raw_buffer_load_b128(rsWS, lane * 16, hb1 + kk * 1024, 16);
      SB();
      WAITV(14);
    } else {
      WAITV(0);
    }
    arrive_flag(L, 1 + stw, 4, ws, FL3 + (T3 * 2 + stw) * 28 + fs3, tag, lane);
    TS(27);
    if (w >= 4) {
      const u32 lp = 2 * (w - 4) * PL;
      __builtin_amdgcn_s_setprio(0);
      await2(L, 1, lane);
      dma_run<7, 1024, 16 * 896>(L0 + lp, vo4, rsWd, so4 + 7 * 16 * 896);
      dma_run<7, 1024, 16 * 896>(L0 + lp + PL, vo4, rsWd, so4 + (7 * 16 + 8) * 896);
      if (w == 4) {
        poll(ws, FL3 + (xcd * 2 + 1) * 28 + lane, lane < 28, tag);
        post(L, 1);
      } else {
        pend(L, 1);
      }
      TS(29);
#pragma unroll
      for (int kk = 0; kk < 14; ++kk) hf[kk] = __builtin_amdgcn_raw_buffer_load_b128(rsWS, lane * 16, hb1 + kk * 1024, 16);
      SB();
      WAITV(14);
#pragma unroll
      for (int k = 0; k < 7; ++k) rd4(7 + k, lp + k * 1024, PL);
    }
    prog(L, w, 14);
    TS(28);
  }
#pragma unroll 1
  for (int st = 1; st >= 0; --st) {
    if (!st) {
      if (w == 4) {
        __builtin_amdgcn_s_sleep(PD3);
        poll(ws, FL3 + xcd * 2 * 28 + lane, lane < 28, tag);
        post(L, 0);
      } else {
        pend(L, 0);
      }
      TS(33);
#pragma unroll
      for (int kk = 0; kk < 14; ++kk) hf[kk] = __builtin_amdgcn_raw_buffer_load_b128(rsWS, lane * 16, hb1 - 4 * 112 * 1024 + kk * 1024, 16);
      SB();
    }
    f32x16 ac4[2];
#pragma unroll
    for (int i = 0; i < 16; ++i) ac4[0][i] = ac4[1][i] = 0.f;
#pragma unroll
    for (int kk = 0; kk < 14; ++kk) {
      ac4[0] = mfma32(w4f[kk][0], hf[kk], ac4[0]);
      ac4[1] = mfma32(w4f[kk][1], hf[kk], ac4[1]);
    }
    TS(34 - 4 * st);
    if (st) await(L, 0xEEEEEEEEu, lane);
    write_part(ac4, L, (1 - st) * 8 + w, lane);
    TS(35 - 4 * st);
  }
  BAR();
  TS(38);
  {
    f32x4 lo = {bflo(hres[0]), bfhi(hres[0]), bflo(hres[1]), bfhi(hres[1])}, hi = {bflo(hres[2]), bfhi(hres[2]), bflo(hres[3]), bfhi(hres[3])};
    const int l2 = __builtin_amdgcn_mbcnt_hi(~0u, __builtin_amdgcn_mbcnt_lo(~0u, 0u)), r2 = l2 & 15, g2 = l2 >> 4;
    const u32 frow2 = (256 * xcd + 2 * (64 * stw + 32 * q2 + 16 * rb + r2) + p2) * 896 + c2 * 128 + ch2 * 64 + g2 * 8;
    LAS char* pa = L + ((1 - stw) * 8 * 32 + 16 * rb + r2) * PROW + (32 * ch2 + 4 * g2) * 4;
#pragma unroll
    for (int k = 0; k < 8; ++k) {
      lo += *(LAS f32x4*)(pa + k * 32 * PROW);
      hi += *(LAS f32x4*)(pa + k * 32 * PROW + 64);
    }
    *(GAS v2u*)((GAS char*)outg + frow2) = v2u{pk(lo[0], lo[1]), pk(lo[2], lo[3])};
    *(GAS v2u*)((GAS char*)outg + frow2 + 32) = v2u{pk(hi[0], hi[1]), pk(hi[2], hi[3])};
    asm volatile(".Lfb_last:");
    TS(39);
    TC(54);
  }
  TSOUT();
}

extern "C" void kernel_launch(void* const* d_in, const int* in_sizes, int n_in, void* d_out, int out_size, void* d_ws,
                              size_t ws_size, hipStream_t stream) {
  fused_block<<<256, 512, 0, stream>>>(d_in[0], d_in[1], d_in[2], d_in[4], d_in[5], d_in[6], d_out, (char*)d_ws);
}
```

```cpp
#include <hip/hip_runtime.h>
#include <hip/hip_bf16.h>

typedef unsigned u32;
typedef unsigned long long u64;
typedef u32 v4u __attribute__((ext_vector_type(4)));
typedef u32 v2u __attribute__((ext_vector_type(2)));
typedef u64 v8q __attribute__((ext_vector_type(8)));
typedef short s16x4 __attribute__((ext_vector_type(4)));
typedef short s16x8 __attribute__((ext_vector_type(8)));
typedef __bf16 bf16x2 __attribute__((ext_vector_type(2)));
typedef __bf16 bf16x8 __attribute__((ext_vector_type(8)));
typedef float f32x2 __attribute__((ext_vector_type(2)));
typedef float f32x4 __attribute__((ext_vector_type(4)));
typedef float f32x16 __attribute__((ext_vector_type(16)));
#define GAS __attribute__((address_space(1)))
#define LAS __attribute__((address_space(3)))
#if defined(__HIP_DEVICE_COMPILE__)
#define NOPK __attribute__((target("no-packed-fp32-ops")))
#else
#define NOPK
#endif
#define DI __device__ __forceinline__ NOPK
typedef __amdgpu_buffer_rsrc_t rsrc_t;
template <int V>
struct IC {
  static constexpr int v = V;
};
template <int A, int B, class F>
DI void sfor(F&& f) {
  if constexpr (B - A == 1) {
    f(IC<A>{});
  } else if constexpr (B - A > 1) {
    sfor<A, (A + B) / 2>(f);
    sfor<(A + B) / 2, B>(f);
  }
}
#define LAMBDA __attribute__((always_inline)) NOPK

#define XB_DELAY 12
#define W3EARLY 10
#define PSLEEP 4
#define PD1 4
#define TRE 2
#define CPF 24
#define PD2 10
#define W4T 8
#define W4H 14
#define W4P 6
#define PD3 4
#define F0 10
#define TS1 40
#define TS2 84
#define LDELAY 14
#define TOUCH1 1
#define TOUCH 1
#define STAMP 0

constexpr u32 WS_FLAG = 4096;
constexpr u32 WS_O = 1024 * 1024;
constexpr u32 WS_H = WS_O + 2 * 1024 * 1024;
constexpr u32 WS_ST = WS_H + 64 * 28 * 1024;
constexpr u32 WS_HH = WS_ST + 256 * 1024;
constexpr u32 WS_END = WS_HH + 64 * 112 * 1024;
constexpr u32 WS_DBG = 16 * 1024 * 1024;
constexpr int FL1 = 0, FL2 = 256, FL3 = 768;

constexpr u32 XROW = 912;
constexpr u32 L_X = 36864;
constexpr u32 L_XH = 153600;
constexpr u32 PL = 14368;
constexpr u32 L_W2 = 6 * PL;
constexpr u32 W2PL = 3616, W2Q = 4 * W2PL;
constexpr u32 L_Q = 0;
constexpr u32 L_K = L_W2 + 4 * W2Q;
constexpr u32 L_V = L_K + 9 * 1024;
constexpr u32 L_RS = L_V + 9 * 1024;
constexpr u32 L_CS1 = L_RS + 544;
constexpr u32 L_TCH = L_CS1 + 384;
constexpr u32 L_PRG = L_TCH + 256;
constexpr u32 L_S4 = 8 * PL;
constexpr u32 L_T4 = L_S4 + 32768;
constexpr u32 L_CS3 = L_RS;
constexpr u32 L_ST3 = L_T4 + 8192;
constexpr u32 PROW = 272;
constexpr u32 LDS_BYTES = 163840;
constexpr int NA4 = 16, NB4 = 12;

#define WAITV_(n) asm volatile("s_waitcnt vmcnt(" #n ")" ::: "memory")
#define WAITV(n) WAITV_(n)
#define BAR() asm volatile("s_waitcnt lgkmcnt(0)\n\ts_barrier" ::: "memory")
#define SB() __builtin_amdgcn_sched_barrier(0)

DI void dma16(u32 lds, u32 voff, rsrc_t rs, u32 soff) {
  asm volatile("s_mov_b32 m0, %0\n\ts_nop 0\n\tbuffer_load_dwordx4 %1, %2, %3 offen lds" ::"s"(lds), "v"(voff), "s"(rs), "s"(soff)
               : "memory");
}
template <int N, int DL, int DS>
DI void dma_run(u32 lds, u32 voff, rsrc_t rs, u32 soff) {
  u32 t;
  asm volatile(
      "s_mov_b32 m0, %1\n\ts_mov_b32 %0, %4\n\t"
      ".rept %c5\n\tbuffer_load_dwordx4 %2, %3, %0 offen lds\n\ts_add_u32 m0, m0, %c6\n\ts_add_u32 %0, %0, %c7\n\t.endr"
      : "=&s"(t)
      : "s"(lds), "v"(voff), "s"(rs), "s"(soff), "i"(N), "i"(DL), "i"(DS)
      : "memory", "scc");
}
template <int N>
DI void dma_run_s(u32 lds, u32 voff, rsrc_t rs, u32 soff, u32 dl, u32 ds) {
  u32 t;
  asm volatile(
      "s_mov_b32 m0, %1\n\ts_mov_b32 %0, %4\n\t"
      ".rept %c5\n\tbuffer_load_dwordx4 %2, %3, %0 offen lds\n\ts_add_u32 m0, m0, %6\n\ts_add_u32 %0, %0, %7\n\t.endr"
      : "=&s"(t)
      : "s"(lds), "v"(voff), "s"(rs), "s"(soff), "i"(N), "s"(dl), "s"(ds)
      : "memory", "scc");
}
DI void dma_loop(u32 lds, u32 voff, rsrc_t rs, u32 soff, u32 dl, u32 ds, u32 nm1) {
  asm volatile(
      "s_mov_b32 m0, %2\n"
      "1:\n\ts_sub_u32 %1, %1, 1\n\tbuffer_load_dwordx4 %3, %4, %0 offen lds\n\ts_cbranch_scc1 2f\n\t"
      "s_add_u32 m0, m0, %5\n\ts_add_u32 %0, %0, %6\n\ts_branch 1b\n2:"
      : "+s"(soff), "+s"(nm1)
      : "s"(lds), "v"(voff), "s"(rs), "s"(dl), "s"(ds)
      : "memory", "scc");
}
DI void dma_x(u32 lds, u32 v0, u32 v1, rsrc_t rs, u32 soff) {
  u32 n, t;
  asm volatile(
      "s_mov_b32 m0, %3\n\ts_mov_b32 %1, 6\n"
      "1:\n\ts_add_u32 %2, %0, 28672\n\tbuffer_load_dwordx4 %4, %6, %0 offen lds\n\ts_add_u32 m0, m0, 1024\n\ts_nop 0\n\t"
      "buffer_load_dwordx4 %5, %6, %0 offen lds\n\ts_add_u32 m0, m0, 1024\n\ts_add_u32 %0, %0, 128\n\t"
      "buffer_load_dwordx4 %4, %6, %2 offen lds\n\ts_add_u32 m0, m0, 1024\n\ts_nop 0\n\t"
      "buffer_load_dwordx4 %5, %6, %2 offen lds\n\ts_add_u32 m0, m0, 1024\n\ts_sub_u32 %1, %1, 1\n\ts_cbranch_scc0 1b"
      : "+s"(soff), "=&s"(n), "=&s"(t)
      : "s"(lds), "v"(v0), "v"(v1), "s"(rs)
      : "memory", "scc");
}
template <int N>
DI void waitv() { asm volatile("s_waitcnt vmcnt(%c0)" ::"i"(N) : "memory"); }
DI void st128(v4u v, rsrc_t rs, u32 voff, u32 soff) {
  __builtin_amdgcn_raw_buffer_store_b128(v, rs, voff, soff, 16);
  asm volatile("s_nop 1" ::"v"(v) : "memory");
}
DI rsrc_t mkrs(const void* p, u32 bytes) { return __builtin_amdgcn_make_buffer_rsrc((void*)p, (short)0, (int)bytes, 0x00020000); }
DI v4u trfrag(LAS char* a, LAS char* b) {
  s16x4 lo = __builtin_amdgcn_ds_read_tr16_b64_v4i16((LAS s16x4*)a);
  s16x4 hi = __builtin_amdgcn_ds_read_tr16_b64_v4i16((LAS s16x4*)b);
  return __builtin_bit_cast(v4u, __builtin_shufflevector(lo, hi, 0, 1, 2, 3, 4, 5, 6, 7));
}
DI f32x4 mfma16(v4u a, v4u b, f32x4 c) {
  return __builtin_amdgcn_mfma_f32_16x16x32_bf16(__builtin_bit_cast(bf16x8, a), __builtin_bit_cast(bf16x8, b), c, 0, 0, 0);
}
DI f32x16 mfma32(v4u a, v4u b, f32x16 c) {
  return __builtin_amdgcn_mfma_f32_32x32x16_bf16(__builtin_bit_cast(bf16x8, a), __builtin_bit_cast(bf16x8, b), c, 0, 0, 0);
}
DI float u2f(u32 u) { return __builtin_bit_cast(float, u); }
DI u32 f2u(float f) { return __builtin_bit_cast(u32, f); }
DI u32 pk(float a, float b) {
  f32x2 f = {a, b};
  return __builtin_bit_cast(u32, __builtin_convertvector(f, bf16x2));
}
DI float bflo(u32 u) { return __builtin_bit_cast(float, u << 16); }
DI float bfhi(u32 u) { return __builtin_bit_cast(float, u & 0xffff0000u); }
DI float dot2(u32 a, u32 b, float c) {
  return __builtin_amdgcn_fdot2_f32_bf16(__builtin_bit_cast(bf16x2, a), __builtin_bit_cast(bf16x2, b), c, false);
}
DI float sum16(float x) {
  auto r = __builtin_amdgcn_permlane16_swap(__builtin_bit_cast(u32, x), __builtin_bit_cast(u32, x), false, false);
  const u32 a = r[0], c = r[1];
  return u2f(a) + u2f(c);
}
DI float sum32(float x) {
  auto r = __builtin_amdgcn_permlane32_swap(__builtin_bit_cast(u32, x), __builtin_bit_cast(u32, x), false, false);
  const u32 a = r[0], c = r[1];
  return u2f(a) + u2f(c);
}
DI float max16(float x) {
  auto r = __builtin_amdgcn_permlane16_swap(__builtin_bit_cast(u32, x), __builtin_bit_cast(u32, x), false, false);
  const u32 a = r[0], c = r[1];
  float m;
  asm("v_max_f32 %0, %1, %2" : "=v"(m) : "v"(a), "v"(c));
  return m;
}
DI float max32(float x) {
  auto r = __builtin_amdgcn_permlane32_swap(__builtin_bit_cast(u32, x), __builtin_bit_cast(u32, x), false, false);
  const u32 a = r[0], c = r[1];
  float m;
  asm("v_max_f32 %0, %1, %2" : "=v"(m) : "v"(a), "v"(c));
  return m;
}
DI float bperm(int srclane, float x) {
  return __builtin_bit_cast(float, __builtin_amdgcn_ds_bpermute(srclane * 4, __builtin_bit_cast(int, x)));
}
#define RLX_AGENT __ATOMIC_RELAXED, __HIP_MEMORY_SCOPE_AGENT
#define RLX_WG __ATOMIC_RELAXED, __HIP_MEMORY_SCOPE_WORKGROUP
#define WAITL() asm volatile("s_waitcnt lgkmcnt(0)" ::: "memory")
DI void poll(char* ws, int idx, bool act, u64 tag) {
  GAS u64* p = (GAS u64*)(ws + WS_FLAG + (u32)idx * 128u);
#pragma unroll 1
  for (int spins = 0; spins < (1 << 16); ++spins) {
    if (act) act = __hip_atomic_load(p, RLX_AGENT) != tag;
    if (__builtin_amdgcn_ballot_w64(act) == 0) break;
    __builtin_amdgcn_s_sleep(PSLEEP);
  }
  asm volatile("" ::: "memory");
}
DI void setflag(char* ws, int idx, u64 tag) { __hip_atomic_store((GAS u64*)(ws + WS_FLAG + (u32)idx * 128u), tag, RLX_AGENT); }
DI void arrive_flag(LAS char* L, int cnt, u32 n, char* ws, int idx, u64 tag, int lane) {
  if (lane == 0) {
    const u32 old = __hip_atomic_fetch_add((LAS u32*)(L + L_PRG + 32 + cnt * 4), 1u, RLX_WG);
    if (old == n - 1) __hip_atomic_store((GAS u64*)(ws + WS_FLAG + (u32)idx * 128u), tag, RLX_AGENT);
  }
}
DI void post(LAS char* L, int k) {
  asm volatile("" ::: "memory");
  *(volatile LAS u32*)(L + L_PRG + 128 + k * 4) = 1u;
  SB();
}
DI void pend(LAS char* L, int k) {
#pragma unroll 1
  for (int spins = 0; spins < (1 << 22); ++spins) {
    if (__builtin_amdgcn_readfirstlane(*(volatile LAS u32*)(L + L_PRG + 128 + k * 4)) != 0) break;
    __builtin_amdgcn_s_sleep(1);
  }
  asm volatile("" ::: "memory");
}
DI void prog(LAS char* L, int w, u32 stage) {
  asm volatile("" ::: "memory");
  *(volatile LAS u32*)(L + L_PRG + w * 4) = stage;
  SB();
}
DI void prog2(LAS char* L, int w, u32 stage) {
  asm volatile("" ::: "memory");
  *(volatile LAS u32*)(L + L_PRG + 96 + w * 4) = stage;
  SB();
}
DI void await2(LAS char* L, u32 stage, int lane) {
#pragma unroll 1
  for (int spins = 0; spins < (1 << 20); ++spins) {
    const u32 v = *(volatile LAS u32*)(L + L_PRG + 96 + (lane & 7) * 4);
    if (__builtin_amdgcn_ballot_w64(v < stage) == 0) break;
    __builtin_amdgcn_s_sleep(1);
  }
  asm volatile("" ::: "memory");
}
DI void await(LAS char* L, u32 need, int lane) {
  const u32 nd = (need >> (4 * (lane & 7))) & 15u;
#pragma unroll 1
  for (int spins = 0; spins < (1 << 20); ++spins) {
    const u32 v = *(volatile LAS u32*)(L + L_PRG + (lane & 7) * 4);
    if (__builtin_amdgcn_ballot_w64(v < nd) == 0) break;
    __builtin_amdgcn_s_sleep(1);
  }
  asm volatile("" ::: "memory");
}
DI void touch(u32 lds, rsrc_t rs, u32 voff, u32 soff) {
  asm volatile("s_mov_b32 m0, %0\n\ts_nop 0\n\tbuffer_load_dword %1, %2, %3 offen sc1 lds" ::"s"(lds), "v"(voff), "s"(rs), "s"(soff)
               : "memory");
}
#define TS(i)
#define TC(i)
#define TSOUT()

DI void pin(LAS char*& p) { asm volatile("" : "+v"(p)); }
DI void pins(u64& m) { asm volatile("" : "+s"(m)); }
DI void write_part(const f32x16 (&acc)[2], LAS char* L, int slot, int lane) {
#pragma unroll
  for (int ct = 0; ct < 2; ++ct)
#pragma unroll
    for (int i = 0; i < 4; ++i)
      *(LAS f32x4*)(L + (slot * 32 + (lane & 31)) * PROW + (32 * ct + 8 * i + 4 * (lane >> 5)) * 4) =
          f32x4{acc[ct][4 * i], acc[ct][4 * i + 1], acc[ct][4 * i + 2], acc[ct][4 * i + 3]};
}

NOPK __global__ __launch_bounds__(512, 2) void fused_block(const void*, const void*, const void*, const void*, const void*, const void*,
                                                           void*, char*) {
  __shared__ __attribute__((aligned(16))) char smem[LDS_BYTES];
  v8q ka;
  asm volatile("s_load_dwordx16 %0, %1, 0x0" : "=s"(ka) : "s"(__builtin_amdgcn_kernarg_segment_ptr()));
  LAS char* L = (LAS char*)smem;
  const u32 L0 = (u32)(size_t)L;
  const u32 tid = __builtin_amdgcn_workitem_id_x();
  const int b = __builtin_amdgcn_workgroup_id_x(), xcd = b & 7, jj = b >> 3;
  const int lane = tid & 63, w = __builtin_amdgcn_readfirstlane(tid >> 6);
  const int r16 = lane & 15, g = lane >> 4, tq = (lane >> 2) & 3, tp = lane & 3, hh = lane >> 5;
  u32 stampv = 0;
  if (tid >= 448 && tid < 456 + CPF) {
    u64 pc;
    u32 clen;
    asm volatile("s_getpc_b64 %0\n.Lfb_here:\n\ts_mov_b32 %1, .Lfb_last-.Lfb_here" : "=s"(pc), "=s"(clen));
    touch(L0 + L_TCH, mkrs((const void*)pc, clen), (lane < 8 ? jj * 8 + lane : lane - 8) * 128, 0);
  }
  if (tid < 264) *(LAS u32*)(L + (tid < 224 ? L_XH : L_PRG - 896) + tid * 4) = tid < 224 ? 0x3f803f80u : 0u;

  const int tile1 = (xcd >> 1) * 4 + (jj & 3), head = (xcd & 1) * 8 + (jj >> 2);
  const int srow0 = 256 * (tile1 >> 1) + (tile1 & 1);
  const u32 vo1 = (lane >> 2) * 2688 + (lane & 3) * 16;
  const u32 so1 = (w - 5) * 896 + ((56 * head) & ~15), l1 = L0 + (w - 5) * 1024;
  const u32 x_l = L0 + L_X + w * 28672, x_s = (srow0 + 64 * w) * 896;
  const u32 x_v0 = (lane >> 3) * 1792 + ((lane & 7) ^ g) * 16, x_v1 = (8 + (lane >> 3)) * 1792 + ((lane & 7) ^ (4 + g)) * 16;
  if (w < 4) __builtin_amdgcn_s_setprio(2);
  asm volatile("s_waitcnt lgkmcnt(0)\n\ts_barrier" : "+s"(ka) : "s"(x_l), "s"(x_s), "v"(x_v0), "v"(x_v1) : "memory");
  const void *xg = (const void*)ka[0], *wqkv = (const void*)ka[1], *wo = (const void*)ka[2], *wg = (const void*)ka[3];
  const void *wu = (const void*)ka[4], *wd = (const void*)ka[5];
  void* outg = (void*)ka[6];
  char* ws = (char*)ka[7];
  const rsrc_t rsX = mkrs(xg, 2048 * 896);
  if (w < 4) {
    dma_x(x_l, x_v0, x_v1, rsX, x_s);
  } else {
    if (w == 4) touch(L0 + L_TCH, rsX, (srow0 + 2 * (16 * (jj >> 2) + (lane >> 2))) * 896 + (3 + (lane & 3)) * 128, 0);
    __builtin_amdgcn_s_sleep(LDELAY);
    if (w > 4 && lane < 32) touch(L0 + L_TCH, mkrs(wqkv, 448 * 2688), (192 + jj * 8 + (lane >> 2)) * 2688 + (w - 5) * 896 + ((xcd & 1) * 3 + (lane & 3)) * 128, 0);
    if (w > 4 || lane < 56)
      dma_loop(w == 4 ? L0 + L_XH + XROW : l1, w == 4 ? lane * 16 : vo1, mkrs(w == 4 ? xg : wqkv, w == 4 ? 2048 * 896 : 448 * 2688),
               w == 4 ? (tile1 < 2 ? 0 : srow0 - 14) * 896 : so1, w == 4 ? XROW : 3072, w == 4 ? 1792 : 16 * 2688, w == 4 ? 6 : 7);
  }
  TS(1);
  TC(48);
  const rsrc_t rsWS = mkrs(ws, WS_END), rsW1 = mkrs(wqkv, 448 * 2688);
  if (w == 4) {
    if (lane < 2) touch(L0 + L_PRG + 64, rsWS, lane * 4, 0);
    touch(L0 + L_TCH, mkrs(wo, 448 * 896), lane < 13 ? b * 1664 + lane * 128 : 0x7fff0000u, 0);
  }
  const rsrc_t rsWo = mkrs(wo, 448 * 896), rsWg = mkrs(wg, 448 * 3584), rsWu = mkrs(wu, 448 * 3584), rsWd = mkrs(wd, 1792 * 896);
  const bool tail = jj < 28;
  const int tt2 = jj & 3, c2 = jj >> 2;
  const int T3 = (xcd >> 2) * 4 + (jj & 3), fs3 = (xcd & 3) * 7 + (jj >> 2);
  const int stw = w >> 2;
  const u32 vo3 = (lane >> 3) * 4 * 3584 + (lane & 7) * 16;
  const int p2 = tt2 >> 1, q2 = tt2 & 1, rb = w & 1, ch2 = (w >> 1) & 1;
  const int k04 = 28 * (w & 3) + (w < 4 ? 14 : 0);
  const u32 vo4 = (lane >> 3) * 896 + (((lane & 7) ^ ((lane >> 2) & 4)) * 16), so4 = 16 * k04 * 896 + c2 * 128;
  v4u w4f[14][2];
  auto load_w4 = [&](int r) LAMBDA { w4f[r >> 1][r & 1] = __builtin_amdgcn_raw_buffer_load_b128(rsWd, vo4, so4 + r * 8 * 896, 0); };
  u64 prm[4], okm[4];
  LAS char *qa, *kaa, *vp, *vo, *raa, *rbb, *rc;
  const u32 oso = WS_O + ((tile1 * 8 + w) * 16 + head) * 1024;
  auto att_setup = [&]() LAMBDA {
    int ln = lane;
    asm volatile("" : "+v"(ln));
    const int r_ = ln & 15, g_ = ln >> 4;
    const bool noprev = (tile1 < 2) && (w == 0);
#pragma unroll
    for (int j = 0; j < 4; ++j) {
      const int d = r_ - (4 * g_ + j);
      prm[j] = __builtin_amdgcn_ballot_w64(d <= -9);
      okm[j] = __builtin_amdgcn_ballot_w64(d <= -9 ? !noprev : (u32)d <= 7u);
    }
    qa = L + L_Q + w * 1024 + ln * 16;
    kaa = L + L_K + w * 1024 + ln * 16;
    LAS char* va = L + L_V + w * 1024 + (ln & 1) * 256 + ((ln >> 2) & 3) * 16 + ((ln >> 1) & 1) * 8;
    vp = va + (g_ | 2) * 64;
    vo = va + 1024 + g_ * 64;
    raa = L + L_RS + (16 * w - 8 + 4 * (g_ | 2)) * 4;
    rbb = L + L_RS + (16 * w + 8 + 4 * g_) * 4;
    rc = L + L_RS + (16 * w + 8 + r_) * 4;
    pin(qa), pin(kaa), pin(vp), pin(vo), pin(raa), pin(rbb), pin(rc);
#pragma unroll
    for (int j = 0; j < 4; ++j) pins(prm[j]), pins(okm[j]);
  };
  if (w < 4) {
    LAS char* wa[2];
    wa[0] = L + (8 * hh + tq) * 64 + ((4 * (g & 1) + tp + (head & 1)) & 7) * 8;
    wa[1] = wa[0] + 42 * 1024;
    LAS char* xa[4];
#pragma unroll
    for (int i = 0; i < 4; ++i) xa[i] = L + L_X + w * 28672 + (lane & 31) * 128 + ((2 * i + hh) ^ ((lane >> 1) & 7)) * 16;
    asm volatile("" : "+v"(wa[0]), "+v"(wa[1]), "+v"(xa[0]), "+v"(xa[1]), "+v"(xa[2]), "+v"(xa[3]));
    v4u xf[28];
    v4u mf = {0u, 0u, 0u, 0u};
    {
      float s1[4] = {0.f, 0.f, 0.f, 0.f}, s2[4] = {0.f, 0.f, 0.f, 0.f};
      sfor<0, 8>([&](auto S_) LAMBDA {
        constexpr int sl = decltype(S_)::v;
        if constexpr (sl < 7) {
          waitv<4 * (6 - sl)>();
#pragma unroll
          for (int i = 0; i < 4; ++i) xf[4 * sl + i] = *(LAS v4u*)(xa[i] + sl * 4096);
          if constexpr (sl == 6) {
            prog(L, w, 2);
            TS(2);
          }
          SB();
        }
        if constexpr (sl >= 1) {
#pragma unroll
          for (int ks = 4 * sl - 4; ks < 4 * sl; ++ks)
#pragma unroll
            for (int e = 0; e < 4; ++e) {
              s1[e] = dot2(xf[ks][e], 0x3f803f80u, s1[e]);
              s2[e] = dot2(xf[ks][e], xf[ks][e], s2[e]);
            }
          SB();
        }
      });
      const float nmean = sum32((s1[0] + s1[1]) + (s1[2] + s1[3])) * (-1.f / 448.f), t2 = sum32((s2[0] + s2[1]) + (s2[2] + s2[3]));
      *(LAS float*)(L + L_RS + (8 + 32 * w + (lane & 31)) * 4) =
          __builtin_amdgcn_rsqf(__builtin_fmaxf(t2 * (1.f / 448.f) - nmean * nmean, 0.f) + 1e-5f);
      const u32 hi = pk(nmean, nmean);
      mf[0] = hh ? 0u : hi;
      mf[1] = hh ? 0u : pk(nmean - bflo(hi), 0.f);
      asm volatile("" : "+v"(mf));
    }
    att_setup();
    TS(8);
    await(L, 0x11100000u, lane);
    TS(7);
    f32x16 acc[3];
#pragma unroll
    for (int m = 0; m < 3; ++m)
#pragma unroll
      for (int i = 0; i < 16; ++i) acc[m][i] = 0.f;
    constexpr int PD = 7;
    v4u wf[PD + 1];
    auto rdw = [&](int n) LAMBDA {
      LAS char* a = wa[n / 42] + (n % 42) * 1024;
      return trfrag(a, a + 256);
    };
#pragma unroll
    for (int n = 0; n < PD; ++n) wf[n] = rdw(n);
    u32 pv = 0, cs[3];
    const u32 need = (0x88803333u >> (4 * (lane & 7))) & 15u;
    LAS char* dq = L + (2 * w + (g & 1)) * 1024 + hh * 256 + r16 * 16;
    LAS char* dk = dq + L_K + 1024;
    asm volatile("" : "+v"(dq), "+v"(dk));
    SB();
    sfor<0, 84>([&](auto N_) LAMBDA {
      constexpr int n = decltype(N_)::v;
      if constexpr ((n + PD + 4) % 12 == 0 && n + PD + 4 < 84) pv = *(volatile LAS u32*)(L + L_PRG + (lane & 7) * 4);
      if constexpr ((n + PD) % 12 == 0 && n + PD < 84) {
        if constexpr ((n + PD) % 24 == 0) prog(L, w, 2 + (n + PD) / 24);
        if (__builtin_amdgcn_ballot_w64((lane & 7) >= 5 && pv < (n + PD) / 12 + 1) != 0) await(L, 0x11100000u * ((n + PD) / 12 + 1), lane);
      }
      if constexpr (n == 78) {
        pv = *(volatile LAS u32*)(L + L_PRG + (lane & 7) * 4);
#pragma unroll
        for (int m = 0; m < 3; ++m) cs[m] = *(volatile LAS u32*)(L + L_CS1 + (32 * m + (lane & 31)) * 4);
      }
      if constexpr (n + PD < 84) wf[(n + PD) % (PD + 1)] = rdw(n + PD);
      acc[n % 3] = mfma32(wf[n % (PD + 1)], xf[n / 3], acc[n % 3]);
      SB();
    });
    TS(3);
    if (__builtin_amdgcn_ballot_w64(pv < need) != 0) {
      await(L, 0x88803333u, lane);
#pragma unroll
      for (int m = 0; m < 3; ++m) cs[m] = *(volatile LAS u32*)(L + L_CS1 + (32 * m + (lane & 31)) * 4);
    }
#pragma unroll
    for (int m = 0; m < 3; ++m) acc[m] = mfma32(v4u{cs[m], cs[m] & 0xffffu, 0u, 0u}, mf, acc[m]);
#pragma unroll
    for (int m = 0; m < 3; ++m) {
      LAS char* d = m == 0 ? dq : dk + (m - 1) * (L_V - L_K);
      *(LAS v4u*)d = v4u{pk(acc[m][0], acc[m][1]), pk(acc[m][2], acc[m][3]), pk(acc[m][4], acc[m][5]), pk(acc[m][6], acc[m][7])};
      const u32 p2 = pk(acc[m][12], acc[m][13]), p3 = pk(acc[m][14], acc[m][15]);
      *(LAS v4u*)(d + 512) = v4u{pk(acc[m][8], acc[m][9]), pk(acc[m][10], acc[m][11]), hh ? 0u : p2, hh ? 0u : p3};
    }
  } else {
    LAS char* xb = L + L_XH + (r16 & 7) * XROW + g * 16;
    v4u xh[14];
    if (w == 4) {
      waitv<TOUCH>();
      prog(L, w, 1);
      __builtin_amdgcn_s_setprio(3);
#pragma unroll
      for (int ks = 0; ks < 14; ++ks) xh[ks] = *(LAS v4u*)(xb + ks * 64);
      float s1[4] = {0.f, 0.f, 0.f, 0.f}, s2[4] = {0.f, 0.f, 0.f, 0.f};
#pragma unroll
      for (int ks = 0; ks < 14; ++ks)
#pragma unroll
        for (int e = 0; e < 4; ++e) {
          s1[e] = dot2(xh[ks][e], 0x3f803f80u, s1[e]);
          s2[e] = dot2(xh[ks][e], xh[ks][e], s2[e]);
        }
      const float nm = sum32(sum16((s1[0] + s1[1]) + (s1[2] + s1[3]))) * (-1.f / 448.f), t2 = sum32(sum16((s2[0] + s2[1]) + (s2[2] + s2[3])));
      *(LAS float*)(L + L_PRG + 96 + (lane & 7) * 4) = nm;
      *(LAS float*)(L + L_RS + (lane & 7) * 4) = __builtin_amdgcn_rsqf(__builtin_fmaxf(t2 * (1.f / 448.f) - nm * nm, 0.f) + 1e-5f);
      prog(L, w, 2);
      await(L, 0x66600000u, lane);
      const u32 tv = lane < 49 ? b * 6272 + lane * 128 : 0x7fff0000u;
      touch(L0 + L_TCH, rsWg, tv, 0);
      touch(L0 + L_TCH, rsWu, tv, 0);
    } else {
      __builtin_amdgcn_s_setprio(3);
      f32x4 h0 = {0.f, 0.f, 0.f, 0.f}, h1 = {0.f, 0.f, 0.f, 0.f};
      LAS char* a0[2];
      LAS char* a1[2];
      a0[0] = L + (3 * (g >> 1) + w - 5) * 1024 + (8 * (g & 1) + tq) * 64 + (tp + (head & 1)) * 8;
      a1[0] = L + (3 * (g >> 1) + w - 5) * 1024 + (8 * (g & 1) + tq) * 64 + ((4 + tp + (head & 1)) & 7) * 8;
      a0[1] = a0[0] + 8 * 6144;
      a1[1] = a1[0] + 8 * 6144;
      asm volatile("" : "+v"(a0[1]), "+v"(a1[1]));
      auto part = [&](auto A_, auto B_) LAMBDA {
        constexpr int A = decltype(A_)::v, B = decltype(B_)::v;
        v4u f[B - A][2];
#pragma unroll
        for (int ks = A; ks < B; ++ks) {
          f[ks - A][0] = trfrag(a0[ks / 8] + (ks % 8) * 6144, a0[ks / 8] + (ks % 8) * 6144 + 256);
          f[ks - A][1] = trfrag(a1[ks / 8] + (ks % 8) * 6144, a1[ks / 8] + (ks % 8) * 6144 + 256);
        }
        SB();
#pragma unroll
        for (int ks = A; ks < B; ++ks) {
          h0 = mfma16(xh[ks], f[ks - A][0], h0);
          h1 = mfma16(xh[ks], f[ks - A][1], h1);
        }
        SB();
      };
      f32x4 hm;
      sfor<0, 7>([&](auto C_) LAMBDA {
        constexpr int c = decltype(C_)::v;
        waitv<(c < 6 ? 4 : 0)>();
        prog(L, w, c + 1);
        if constexpr (c == 0 || c == 1 || c == 3 || c == 6) TS(c == 0 ? 10 : (c == 1 ? 11 : (c == 3 ? 12 : 13)));
        if constexpr (c == 1) await(L, 0x00002222u, lane);
        if constexpr (c < 5) dma_run<4, 3072, 16 * 2688>(l1 + (8 + 4 * c) * 3072, vo1, rsW1, so1 + (8 + 4 * c) * 16 * 2688);
        if constexpr (c == 0) {
          await(L, 0x00010000u, lane);
#pragma unroll
          for (int ks = 0; ks < 14; ++ks) xh[ks] = *(LAS v4u*)(xb + ks * 64);
        }
        part(IC<2 * c>{}, IC<2 * c + 2>{});
        if constexpr (c == 5) {
          await(L, 0x22220000u, lane);
          hm = *(LAS f32x4*)(L + L_PRG + 96 + (g & 1) * 16);
        }
      });
      const u32 e0 = pk(h0[0], h0[0]), e1 = pk(h1[0], h1[0]);
      if (g == 0) {
        *(LAS u32*)(L + L_CS1 + ((w - 5) * 32 + r16) * 4) = pk(bflo(e0), h0[0] - bflo(e0));
        *(LAS u32*)(L + L_CS1 + ((w - 5) * 32 + 16 + r16) * 4) = pk(bflo(e1), h1[0] - bflo(e1));
      }
      prog(L, w, 8);
      if (w != 5) {
        auto r0 = __builtin_amdgcn_permlane16_swap(f2u(h0[0]), f2u(h0[0]), false, false);
        auto r1 = __builtin_amdgcn_permlane16_swap(f2u(h1[0]), f2u(h1[0]), false, false);
        const u32 c0u = r0[0], c1u = r1[0];
        LAS char* d = L + (w == 6 ? L_K : L_V) + ((r16 >> 2) & 1) * 256 + (8 + 4 * (g & 1)) * 16 + (r16 >> 3) * 8 + (r16 & 3) * 2;
#pragma unroll
        for (int j = 0; j < 4; ++j) {
          const u32 t = pk(h0[j] + hm[j] * u2f(c0u), r16 < 12 ? h1[j] + hm[j] * u2f(c1u) : 0.f);
          *(LAS unsigned short*)(d + j * 16) = (unsigned short)t;
          *(LAS unsigned short*)(d + j * 16 + 512) = (unsigned short)(t >> 16);
        }
      }
    }
    att_setup();
    if (tail) {
      __builtin_amdgcn_s_setprio(0);
      await(L, 0x77704444u, lane);
      const u32 vo2 = (lane >> 3) * 4 * 896 + (lane & 7) * 16;
      const u32 l2 = L0 + L_W2 + (w - 4) * W2Q, s2 = 112 * (w - 4) * 896 + c2 * 128;
      dma_run<4, W2PL, 896>(l2, vo2, rsWo, s2);
      dma_run<4, W2PL, 896>(l2 + 1024, vo2, rsWo, s2 + 32 * 896);
      dma_run<4, W2PL, 896>(l2 + 2048, vo2, rsWo, s2 + 64 * 896);
      if (lane < 32) dma_run<4, W2PL, 896>(l2 + 3072, vo2, rsWo, s2 + 96 * 896);
      dma_run<7, 1024, 32 * 3584>(L0 + (1 + ch2) * PL + rb * 7168, vo3, rsWg, (1 + ch2 + rb * 224) * 3584 + fs3 * 128);
#pragma unroll
      for (int r = 0; r < W4P; ++r) load_w4(r);
      SB();
    }
    TS(3);
  }
  __builtin_amdgcn_s_setprio(0);
  u64 tag;
  {
    BAR();
    TS(4);
    const v4u qf = *(LAS v4u*)qa, kp = *(LAS v4u*)kaa, kf = *(LAS v4u*)(kaa + 1024);
    const f32x4 rp = *(LAS f32x4*)raa, ro = *(LAS f32x4*)rbb;
    const float rq = *(LAS float*)rc * (0.18898223650461362f * 1.4426950408889634f);
    const v4u v0 = trfrag(vp, vo), v1 = trfrag(vp + 512, vo + 512);
    const v2u epoch = *(LAS v2u*)(L + L_PRG + 64);
    SB();
    const f32x4 z = {0.f, 0.f, 0.f, 0.f};
    const f32x4 sp = mfma16(kp, qf, z), so = mfma16(kf, qf, z);
    float p[4], rk[4];
    bool pr[4], ok[4];
#pragma unroll
    for (int j = 0; j < 4; ++j) {
      pr[j] = __builtin_amdgcn_inverse_ballot_w64(prm[j]);
      ok[j] = __builtin_amdgcn_inverse_ballot_w64(okm[j]);
      rk[j] = pr[j] ? rp[j] : ro[j];
      p[j] = ok[j] ? (pr[j] ? sp[j] : so[j]) * (rk[j] * rq) : -3.0e38f;
    }
    const float mx = max32(max16(__builtin_fmaxf(__builtin_fmaxf(p[0], p[1]), __builtin_fmaxf(p[2], p[3]))));
#pragma unroll
    for (int j = 0; j < 4; ++j) p[j] = __builtin_amdgcn_exp2f(p[j] - mx);
    float sum = (p[0] + p[1]) + (p[2] + p[3]);
#pragma unroll
    for (int j = 0; j < 4; ++j) p[j] *= rk[j];
    const v4u pf = {pk(pr[0] ? p[0] : 0.f, pr[1] ? p[1] : 0.f), pk(pr[2] ? p[2] : 0.f, pr[3] ? p[3] : 0.f),
                    pk(pr[0] ? 0.f : p[0], pr[1] ? 0.f : p[1]), pk(pr[2] ? 0.f : p[2], pr[3] ? 0.f : p[3])};
    const f32x4 o0 = mfma16(v0, pf, z), o1 = mfma16(v1, pf, z);
    sum = sum32(sum16(sum));
    const float inv = __builtin_amdgcn_rcpf(sum);
    const v4u of = {pk(o0[0] * inv, o0[1] * inv), pk(o0[2] * inv, o0[3] * inv), pk(o1[0] * inv, o1[1] * inv), pk(o1[2] * inv, o1[3] * inv)};
    st128(of, rsWS, lane * 16, oso);
    TS(5);
    const u32 lo = __builtin_amdgcn_readfirstlane(epoch[0]), hi = __builtin_amdgcn_readfirstlane(epoch[1]);
    tag = (((u64)hi << 32) | lo) * 6364136223846793005ull + 1442695040888963407ull;
  }
  if (tail && w >= 2) {
    dma_run<7, 1024, 32 * 3584>(L0 + (2 + (w >> 1)) * PL + rb * 7168, vo3, w < 4 ? rsWg : rsWu, ((w < 4 ? 3 : ch2) + rb * 224) * 3584 + fs3 * 128);
    WAITV(7);
  } else {
    WAITV(0);
  }
  asm volatile("s_barrier" ::: "memory");
  if (tid == 0) setflag(ws, FL1 + tile1 * 16 + head, tag);
  TS(6);
  if (!tail) {
#pragma unroll 1
    for (int k = 0; k < 5; ++k) {
      const u32 idx = (k * 8 + w) * 64 + lane;
      touch(L0 + L_TCH, rsWd, idx < 2352 ? ((448 * (jj - 28) + 112) * 7 + idx) * 128 : 0x7fff0000u, 0);
    }
    if (b == 255 && w < 4) {
      poll(ws, FL1 + w * 64 + lane, true, tag);
      if (lane == 0) {
        u32 old = __hip_atomic_fetch_add((LAS u32*)(L + L_PRG + 32), 1u, RLX_WG);
        if (old == 3) __hip_atomic_store((GAS u64*)ws, tag, RLX_AGENT);
      }
    }
    TSOUT();
    return;
  }

  const u32 lb = w < 4 ? L_S4 + w * 8192 : L_S4 + 16384 + (w - 4) * 2048;
  LAS char* ta0 = L + (4 * hh + tq) * 128 + (tq >> 1) * 64 + (g & 1) * 32 + tp * 8;
  LAS char* ta1 = L + (4 * hh + tq) * 128 + ((tq >> 1) ^ 1) * 64 + (g & 1) * 32 + tp * 8;
  auto tr_w4 = [&](int k) LAMBDA {
    asm volatile("" ::: "memory");
    *(LAS v4u*)(L + lb + lane * 16) = w4f[k][0];
    *(LAS v4u*)(L + lb + 1024 + lane * 16) = w4f[k][1];
    asm volatile("" ::: "memory");
    w4f[k][0] = trfrag(ta0 + lb, ta0 + lb + 1024);
    w4f[k][1] = trfrag(ta1 + lb, ta1 + lb + 1024);
  };
  {
    v4u hres;
    const int blk = (xcd * 2 + p2) * 8 + 4 * stw + 2 * q2 + rb;
    v4u of[8], w2f[8][4];
    auto read_w2 = [&]() LAMBDA {
      LAS char* a = L + L_W2 + 2 * ch2 * W2Q + tq * W2PL + g * 128 + tp * 8;
      LAS char* b0[2] = {a + ch2 * 64, a + (ch2 ^ 1) * 64};
      LAS char* b1[2] = {b0[0] + (g < 3 ? 512 : 0), b0[1] + (g < 3 ? 512 : 0)};
#pragma unroll
      for (int hd = 0; hd < 8; ++hd)
#pragma unroll
        for (int cb = 0; cb < 4; ++cb) {
          const u32 o = (hd >> 2) * W2Q + (hd & 3) * 896 + (cb & 1) * 32;
          w2f[hd][cb] = trfrag(b0[cb >> 1] + o, b1[cb >> 1] + o);
        }
    };
    if (w >= 4) {
#pragma unroll
      for (int r = W4P; r < W4H; ++r) load_w4(r);
      SB();
    } else if (w < 2) {
      dma_run<4, 1024, 8 * 1792>(L0 + L_T4 + w * 4096, (lane >> 3) * 1792 + ((lane & 7) ^ (lane >> 3)) * 16, rsX,
                                 (256 * xcd + 2 * (64 * w + 32 * q2) + p2) * 896 + c2 * 128);
    } else {
      dma_run<7, 1024, 32 * 3584>(L0 + rb * 7168, vo3, rsWg, rb * 224 * 3584 + fs3 * 128);
    }
    read_w2();
    TS(16);
    if (w == 0) {
      __builtin_amdgcn_s_sleep(PD1);
      poll(ws, FL1 + (xcd * 2 + p2) * 16 + lane, lane < 16, tag);
    }
    if (w < 2) WAITV(0);
    BAR();
    TS(17);
#pragma unroll
    for (int hd = 0; hd < 8; ++hd)
      of[hd] = __builtin_amdgcn_raw_buffer_load_b128(rsWS, lane * 16, WS_O + (blk * 16 + 8 * ch2 + hd) * 1024, 16);
    SB();
    LAS char* rrow = L + L_T4 + (32 * stw + 16 * rb + r16) * 128 + (g & 1) * 8;
    LAS char *ra = rrow + ((4 * ch2 + (g >> 1)) ^ (r16 & 7)) * 16, *rb2 = rrow + ((4 * ch2 + 2 + (g >> 1)) ^ (r16 & 7)) * 16;
    const v2u xra = *(LAS v2u*)ra, xrb = *(LAS v2u*)rb2;
    if (w >= 4) {
#pragma unroll
      for (int k = 0; k < TRE; ++k) tr_w4(k);
    }
    SB();
    f32x4 a0 = {bflo(xra[0]), bfhi(xra[0]), bflo(xra[1]), bfhi(xra[1])}, a1 = {bflo(xrb[0]), bfhi(xrb[0]), bflo(xrb[1]), bfhi(xrb[1])};
    f32x4 a2 = {0.f, 0.f, 0.f, 0.f}, a3 = {0.f, 0.f, 0.f, 0.f};
#pragma unroll
    for (int hd = 0; hd < 8; ++hd) {
      a2 = mfma16(w2f[hd][2], of[hd], a2);
      a3 = mfma16(w2f[hd][3], of[hd], a3);
      a0 = mfma16(w2f[hd][0], of[hd], a0);
      a1 = mfma16(w2f[hd][1], of[hd], a1);
    }
    *(LAS f32x4*)(L + L_S4 + w * 2048 + lane * 16) = a2;
    *(LAS f32x4*)(L + L_S4 + w * 2048 + 1024 + lane * 16) = a3;
    BAR();
    a0 += *(LAS f32x4*)(L + L_S4 + (w ^ 2) * 2048 + lane * 16);
    a1 += *(LAS f32x4*)(L + L_S4 + (w ^ 2) * 2048 + 1024 + lane * 16);
    TS(18);
    hres = v4u{pk(a0[0], a0[1]), pk(a0[2], a0[3]), pk(a1[0], a1[1]), pk(a1[2], a1[3])};
    const int t32 = (xcd * 2 + stw) * 4 + tt2;
    st128(hres, rsWS, (g >> 1) * 1024 + ((g & 1) * 32 + 16 * rb + r16) * 16, WS_H + (t32 * 28 + 4 * c2 + 2 * ch2) * 1024);
    float s1 = 0.f, s2 = 0.f;
#pragma unroll
    for (int i = 0; i < 4; ++i) {
      const float lo = bflo(hres[i]), hi = bfhi(hres[i]);
      s1 += lo + hi;
      s2 += lo * lo + hi * hi;
    }
    s1 = sum32(sum16(s1));
    s2 = sum32(sum16(s2));
    if (lane < 16)
      __builtin_amdgcn_raw_buffer_store_b64(v2u{f2u(s1), f2u(s2)}, rsWS, (16 * rb + r16) * 8, WS_ST + (t32 * 14 + 2 * c2 + ch2) * 256, 16);
    prog2(L, w, 0);
    TS(19);
    *(LAS v2u*)ra = v2u{hres[0], hres[1]};
    *(LAS v2u*)rb2 = v2u{hres[2], hres[3]};
    if (w >= 4) {
#pragma unroll
      for (int k = TRE; k < 7; ++k) tr_w4(k);
    }
    asm volatile("s_waitcnt vmcnt(0) lgkmcnt(0)\n\ts_barrier" ::: "memory");
    if (tid == 0) setflag(ws, FL2 + xcd * 28 + jj, tag);
    TS(20);
  }
  auto rd4 = [&](int k, u32 o, u32 d) LAMBDA {
    w4f[k][0] = trfrag(ta0 + o, ta0 + o + d);
    w4f[k][1] = trfrag(ta1 + o, ta1 + o + d);
  };
  if (w < 4) {
    dma_run<7, 1024, 32 * 3584>(L0 + (6 + ch2) * PL + rb * 7168, vo3, rsWu, (2 + ch2 + rb * 224) * 3584 + fs3 * 128);
    dma_run<8, 1024, 8 * 896>(L0 + lb, vo4, rsWd, so4);
    WAITV(8);
    prog(L, w, 11);
    __builtin_amdgcn_s_setprio(3);
    await(L, 0x0000BBBBu, lane);
    {
      f32x4 cg = {0.f, 0.f, 0.f, 0.f}, cu = {0.f, 0.f, 0.f, 0.f};
      const v4u ones = {0x3f803f80u, 0x3f803f80u, 0x3f803f80u, 0x3f803f80u};
      LAS char* ag = L + tq * PL + g * 128 + tp * 8 + w * 32;
      LAS char* au = ag + 4 * PL;
      asm volatile("" : "+v"(au));
      v4u f[6];
      sfor<0, 34>([&](auto N_) LAMBDA {
        constexpr int n = decltype(N_)::v;
        if constexpr (n >= 6) {
          if constexpr (n & 1) cu = mfma16(ones, f[n % 6], cu);
          else cg = mfma16(ones, f[n % 6], cg);
        }
        if constexpr (n < 28) {
          LAS char* a = ((n & 1) ? au : ag) + (n >> 1) * 1024;
          f[n % 6] = trfrag(a, a + 512);
        }
        SB();
      });
      const float c = (g & 1) ? cu[0] : cg[0];
      const u32 hi = pk(c, c);
      *(LAS u32*)(L + L_CS3 + ((lane & 31) * 4 + w) * 4) = pk(bflo(hi), c - bflo(hi));
    }
    __builtin_amdgcn_s_setprio(1);
    WAITV(0);
#pragma unroll
    for (int k = 0; k < 4; ++k) rd4(k, lb + k * 2048, 1024);
    await(L, 0xBBBB0000u, lane);
    {
      v4u sv[2][4];
#pragma unroll
      for (int t = 0; t < 2; ++t)
#pragma unroll
        for (int i = 0; i < 4; ++i)
          sv[t][i] = __builtin_amdgcn_raw_buffer_load_b128(rsWS, (i == 3 && hh) ? 0x7fff0000u : lane * 16,
                                                           WS_ST + (T3 * 8 + w + 4 * t) * 14 * 256 + i * 1024, 16);
      SB();
      dma_run<8, 1024, 8 * 896>(L0 + lb, vo4, rsWd, so4 + 4 * 16 * 896);
#pragma unroll
      for (int t = 0; t < 2; ++t) {
        float q[4];
#pragma unroll
        for (int e = 0; e < 4; ++e) q[e] = sum32(sum16((u2f(sv[t][0][e]) + u2f(sv[t][1][e])) + (u2f(sv[t][2][e]) + u2f(sv[t][3][e]))));
        const float nm = ((g & 1) ? q[2] : q[0]) * (-1.f / 448.f), sq = (g & 1) ? q[3] : q[1];
        const float var = __builtin_fmaxf(sq * (1.f / 448.f) - nm * nm, 0.f) + 1e-5f, rstd = __builtin_amdgcn_rsqf(var);
        const u32 hi = pk(nm, nm);
        *(LAS v4u*)(L + L_ST3 + ((w + 4 * t) * 32 + 2 * r16 + (g & 1)) * 16) =
            v4u{hi, pk(nm - bflo(hi), 0.f), f2u(-rstd * 1.4426950408889634f), f2u(var * rstd)};
      }
      prog(L, w, 12);
    }
    WAITV(0);
#pragma unroll
    for (int k = 4; k < 8; ++k) rd4(k, lb + (k & 3) * 2048, 1024);
    WAITL();
    dma_run<8, 1024, 8 * 896>(L0 + lb, vo4, rsWd, so4 + 8 * 16 * 896);
    TS(22);
    await(L, 0xCCCC0000u, lane);
  } else {
    __builtin_amdgcn_s_setprio(2);
    if (w == 4) {
      __builtin_amdgcn_s_sleep(PD2);
      poll(ws, FL2 + T3 * 28 + lane, lane < 28, tag);
      post(L, 2);
    } else {
      pend(L, 2);
    }
  }

  v4u hf[14];
  const u32 hb1 = WS_HH + ((xcd * 8 + 4 + tt2) * 112 + k04) * 1024;
  {
    TS(24);
    TC(49);
    const int t32 = T3 * 8 + w;
    LAS char* a3 = L + (4 * (g & 1) + tq) * PL + hh * 128 + tp * 8;
    constexpr int PD = 4, HD = 14, H0 = 12;
    v4u hb[HD];
#pragma unroll
    for (int ks = 0; ks < H0; ++ks) hb[ks] = __builtin_amdgcn_raw_buffer_load_b128(rsWS, lane * 16, WS_H + (t32 * 28 + ks) * 1024, 16);
    SB();
    if (w >= 4) {
      prog(L, w, 11);
      await(L, 0x0000BBBBu, lane);
    } else {
      waitv<4 + H0>();
      rd4(8, lb, 1024);
      rd4(9, lb + 2048, 1024);
      WAITL();
      dma_run<4, 1024, 8 * 896>(L0 + lb, vo4, rsWd, so4 + 12 * 16 * 896);
      await(L, 13u << (4 * (w + 4)), lane);
    }
    TS(21);
    f32x16 acc[4];
#pragma unroll
    for (int c = 0; c < 4; ++c)
#pragma unroll
      for (int i = 0; i < 16; ++i) acc[c][i] = 0.f;
    v4u wf[PD + 1], csv;
    auto rdw = [&](int n) LAMBDA {
      const int ks = n >> 2, c = n & 3;
      if (ks == 28) return v4u{csv[c], csv[c] & 0xffffu, 0u, 0u};
      const u32 o = (ks >> 1) * 1024 + (ks & 1) * 256 + c * 32;
      return trfrag(a3 + o, a3 + o + 512);
    };
#pragma unroll
    for (int n = 0; n < PD; ++n) wf[n] = rdw(n);
    v4u stv;
    v4u mf = {0u, 0u, 0u, 0u};
    u32 pv = 0;
    SB();
    sfor<0, 116>([&](auto N_) LAMBDA {
      constexpr int n = decltype(N_)::v, S0 = 72, ks = n >> 2, c = n & 3;
      if (n == 4) TC(50);
      if (n == 58) TC(51);
      if constexpr (n + PD == 56) prog2(L, w, 1);
      if constexpr (n == TS1) prog(L, w, 12);
      if constexpr (n == TS2) prog(L, w, 13);
      if constexpr (n == 92) pv = *(volatile LAS u32*)(L + L_PRG + (lane & 3) * 4);
      if constexpr (n == 97)
        if (__builtin_amdgcn_ballot_w64(pv < 12) != 0) await(L, 0x0000CCCCu, lane);
      if constexpr (n == 100) csv = *(LAS v4u*)(L + L_CS3 + (lane & 31) * 16);
      if constexpr (n + PD < 116) wf[(n + PD) % (PD + 1)] = rdw(n + PD);
      if constexpr (ks == 28) acc[c] = mfma32(wf[n % (PD + 1)], mf, acc[c]);
      else acc[c] = mfma32(wf[n % (PD + 1)], hb[ks % HD], acc[c]);
      if constexpr (c == 3 && H0 + ks < 28)
        hb[(H0 + ks) % HD] = __builtin_amdgcn_raw_buffer_load_b128(rsWS, lane * 16, WS_H + (t32 * 28 + H0 + ks) * 1024, 16);
      if constexpr (n == 102) stv = *(LAS v4u*)(L + L_ST3 + (w * 32 + (lane & 31)) * 16);
      if constexpr (n == 108) {
        mf[0] = hh ? 0u : stv[0];
        mf[1] = hh ? 0u : stv[1];
      }
      SB();
    });
    TS(25);
    TC(52);
    const float ng = u2f(stv[2]), irs = u2f(stv[3]);
#pragma unroll
    for (int c = 0; c < 4; ++c) {
      float o[8];
#pragma unroll
      for (int i = 0; i < 8; ++i) o[i] = acc[c][8 + i] * __builtin_amdgcn_rcpf(__builtin_amdgcn_exp2f(acc[c][i] * ng) * irs + irs);
      st128(v4u{pk(o[0], o[1]), pk(o[2], o[3]), pk(o[4], o[5]), pk(o[6], o[7])}, rsWS, lane * 16, WS_HH + (t32 * 112 + fs3 * 4 + c) * 1024);
    }
    TS(26);
    TC(53);
    if (w < 4) {
#pragma unroll
      for (int k = 10; k < 14; ++k) rd4(k, lb + (k & 3) * 2048, 1024);
      pend(L, 1);
#pragma unroll
      for (int kk = 0; kk < 14; ++kk) hf[kk] = __builtin_amdgcn_raw_buffer_load_b128(rsWS, lane * 16, hb1 + kk * 1024, 16);
      SB();
      WAITV(14);
    } else {
      WAITV(0);
    }
    arrive_flag(L, 1 + stw, 4, ws, FL3 + (T3 * 2 + stw) * 28 + fs3, tag, lane);
    TS(27);
    if (w >= 4) {
      const u32 lp = 2 * (w - 4) * PL;
      __builtin_amdgcn_s_setprio(0);
      await2(L, 1, lane);
      dma_run<7, 1024, 16 * 896>(L0 + lp, vo4, rsWd, so4 + 7 * 16 * 896);
      dma_run<7, 1024, 16 * 896>(L0 + lp + PL, vo4, rsWd, so4 + (7 * 16 + 8) * 896);
      if (w == 4) {
        poll(ws, FL3 + (xcd * 2 + 1) * 28 + lane, lane < 28, tag);
        post(L, 1);
      } else {
        pend(L, 1);
      }
      TS(29);
#pragma unroll
      for (int kk = 0; kk < 14; ++kk) hf[kk] = __builtin_amdgcn_raw_buffer_load_b128(rsWS, lane * 16, hb1 + kk * 1024, 16);
      SB();
      WAITV(14);
#pragma unroll
      for (int k = 0; k < 7; ++k) rd4(7 + k, lp + k * 1024, PL);
    }
    prog(L, w, 14);
    TS(28);
  }
  auto mma4 = [&](int slot, bool late) LAMBDA {
    f32x16 ac4[2];
#pragma unroll
    for (int i = 0; i < 16; ++i) ac4[0][i] = ac4[1][i] = 0.f;
#pragma unroll
    for (int kk = 0; kk < 14; ++kk) {
      ac4[0] = mfma32(w4f[kk][0], hf[kk], ac4[0]);
      ac4[1] = mfma32(w4f[kk][1], hf[kk], ac4[1]);
      if (late) {
        if (kk + F0 < 14) hf[kk + F0] = __builtin_amdgcn_raw_buffer_load_b128(rsWS, lane * 16, hb1 - 4 * 112 * 1024 + (kk + F0) * 1024, 16);
        SB();
      }
    }
    if (!late) await(L, 0xEEEEEEEEu, lane);
    write_part(ac4, L, slot, lane);
  };
  const int l2 = __builtin_amdgcn_mbcnt_hi(~0u, __builtin_amdgcn_mbcnt_lo(~0u, 0u)), q4 = l2 & 15, tk = 4 * w + (l2 >> 4);
  auto finish = [&](int st) LAMBDA {
    const v2u rs = *(LAS v2u*)(L + L_T4 + (32 * st + tk) * 128 + ((q4 >> 1) ^ (tk & 7)) * 16 + (q4 & 1) * 8);
    f32x4 s0 = {bflo(rs[0]), bfhi(rs[0]), bflo(rs[1]), bfhi(rs[1])}, s1 = {0.f, 0.f, 0.f, 0.f};
    LAS char* pa = L + ((1 - st) * 8 * 32 + tk) * PROW + q4 * 16;
#pragma unroll
    for (int k = 0; k < 8; k += 2) {
      s0 += *(LAS f32x4*)(pa + k * 32 * PROW);
      s1 += *(LAS f32x4*)(pa + (k + 1) * 32 * PROW);
    }
    s0 += s1;
    const u32 fr = (256 * xcd + 2 * (64 * st + 32 * q2 + tk) + p2) * 896 + c2 * 128 + q4 * 8;
    *(GAS v2u*)((GAS char*)outg + fr) = v2u{pk(s0[0], s0[1]), pk(s0[2], s0[3])};
  };
  mma4(w, false);
  prog(L, w, 15);
  TS(31);
  if (w == 4) {
    __builtin_amdgcn_s_sleep(PD3);
    poll(ws, FL3 + xcd * 2 * 28 + lane, lane < 28, tag);
    post(L, 0);
  }
  await(L, 0xFFFFFFFFu, lane);
  finish(1);
  SB();
  pend(L, 0);
  TS(33);
#pragma unroll
  for (int kk = 0; kk < F0; ++kk) hf[kk] = __builtin_amdgcn_raw_buffer_load_b128(rsWS, lane * 16, hb1 - 4 * 112 * 1024 + kk * 1024, 16);
  SB();
  TS(34);
  mma4(8 + w, true);
  TS(35);
  BAR();
  TS(38);
  finish(0);
  asm volatile(".Lfb_last:");
  TS(39);
  TC(54);
  TSOUT();
}

extern "C" void kernel_launch(void* const* d_in, const int* in_sizes, int n_in, void* d_out, int out_size, void* d_ws,
                              size_t ws_size, hipStream_t stream) {
  fused_block<<<256, 512, 0, stream>>>(d_in[0], d_in[1], d_in[2], d_in[4], d_in[5], d_in[6], d_out, (char*)d_ws);
}
```
